```python
import torch

def _softmax(x, axis=-1):
    return torch.softmax(x, dim=axis)
def _gelu(x):
    return torch.nn.functional.gelu(x, approximate="tanh")
def _silu(x):
    return torch.nn.functional.silu(x)
def _sigmoid(x):
    return torch.sigmoid(x)
def _layernorm(x, axis=-1, eps=1e-5):
    m = x.mean(dim=axis, keepdim=True)
    v = x.var(dim=axis, keepdim=True, unbiased=False)
    return (x - m) / torch.sqrt(v + eps)
def _scatter_add(tgt, idx, src, axis=0):
    return tgt.clone().index_add_(axis, idx, src)
def _segment_sum(src, seg, n):
    out = torch.zeros((n,) + src.shape[1:], dtype=src.dtype)
    return out.index_add_(0, seg, src)
def _pad(x, pw):
    flat = tuple(v for pair in reversed(pw) for v in pair)
    return torch.nn.functional.pad(x, flat)

kernel_name = 'sequential_preLN__sliding_block_cached__dense_activation_silu__decode'

def setup_inputs():

    _w = (lambda a: (lambda t: t.to(torch.bfloat16) if t.is_floating_point() else t)(torch.as_tensor(a)))
    def _A_setup():
        B, Sq, D = 2, 1, 512
        Hq, Hkv, Sk, Dh = 32, 8, 8192, 128
        assert Hq % Hkv == 0, (Hq, Hkv)
        return {
            "x": _w((lambda *s: torch.randn(*s, dtype=torch.bfloat16) * 0.05)(B, Sq, D)),
            "k_cache": _w((lambda *s: torch.randn(*s, dtype=torch.bfloat16) * 3.2)(B, Hkv, Sk, Dh)),
            "v_cache": _w((lambda *s: torch.randn(*s, dtype=torch.bfloat16) * 0.8)(B, Hkv, Sk, Dh)),
            "wq": _w((lambda *s: torch.randn(*s, dtype=torch.bfloat16) * 3.2)(D, Hq * Dh) * (D ** -0.5)),
            "wo": _w((lambda *s: torch.randn(*s, dtype=torch.bfloat16) * 0.8)(Hq * Dh, D) * ((Hq * Dh) ** -0.5)),
        }
    _A_in = _A_setup()

    def _B_setup():
        B, S, D, F = 2, 1, 512, 1792
        return {
            "x": _w((lambda *s: torch.randn(*s, dtype=torch.bfloat16) * 0.05)(B, S, D)),
            "w1": _w((lambda *s: torch.randn(*s, dtype=torch.bfloat16) * 3.2)(D, F) * (D ** -0.5)),
            "w2": _w((lambda *s: torch.randn(*s, dtype=torch.bfloat16) * 0.4)(F, D) * (F ** -0.5)),
        }
    _B_in = _B_setup()

    return {
        "A_x": _A_in["x"],
        "A_k_cache": _A_in["k_cache"],
        "A_v_cache": _A_in["v_cache"],
        "A_wq": _A_in["wq"],
        "A_wo": _A_in["wo"],
        "B_x": _B_in["x"],
        "B_w1": _B_in["w1"],
        "B_w2": _B_in["w2"],
    }

def reference(A_x, A_k_cache, A_v_cache, A_wq, A_wo, B_x, B_w1, B_w2):
    def _A_ref(x, k_cache, v_cache, wq, wo):
        B, Sq, D = x.shape
        Hq, Dh = 32, 128
        Hkv, Sk = k_cache.shape[1], k_cache.shape[2]
        W = min(4096, max(256, Sk // 4))
        rep = Hq // Hkv
        q = (lambda x, *d: x.permute(*d))((x @ wq).reshape(B, Sq, Hq, Dh), 0, 2, 1, 3)
        k = (lambda x, n, axis: torch.repeat_interleave(x, n, dim=axis))(k_cache[:, :, Sk - W:, :], rep, axis=1)
        v = (lambda x, n, axis: torch.repeat_interleave(x, n, dim=axis))(v_cache[:, :, Sk - W:, :], rep, axis=1)
        scores = torch.einsum("bhqd,bhkd->bhqk", q, k) * (Dh ** -0.5)
        p = _softmax(scores, axis=-1)
        o = torch.einsum("bhqk,bhkd->bhqd", p, v)
        return (lambda x, *d: x.permute(*d))(o, 0, 2, 1, 3).reshape(B, Sq, Hq * Dh) @ wo

    def _B_ref(x, w1, w2):
        h = torch.einsum("bsd,df->bsf", x, w1)
        h = _silu(h)
        out = torch.einsum("bsf,fd->bsd", h, w2)
        return out

    x = A_x
    h = x + _A_ref(_layernorm(x, axis=-1), A_k_cache, A_v_cache, A_wq, A_wo)
    h2 = h + _B_ref(_layernorm(h, axis=-1), B_w1, B_w2)
    return h2
```

```cpp
#include <hip/hip_runtime.h>
#include <hip/hip_bf16.h>

#define EMU 0

typedef unsigned short u16;
__device__ __forceinline__ float bf2f(u16 b) { return __uint_as_float(((unsigned)b) << 16); }
__device__ __forceinline__ u16 f2bf(float f) { unsigned u = __float_as_uint(f); u += 0x7fffu + ((u >> 16) & 1u); return (u16)(u >> 16); }
__device__ __forceinline__ float rb(float f) { return EMU ? bf2f(f2bf(f)) : f; }

constexpr int B = 2, D = 512, HQ = 32, HKV = 8, SK = 8192, DH = 128, W = 2048, F = 1792;

__device__ float block_sum(float v, float* sh) {
    for (int o = 32; o >= 1; o >>= 1) v += __shfl_xor(v, o);
    int w = threadIdx.x >> 6;
    __syncthreads();
    if ((threadIdx.x & 63) == 0) sh[w] = v;
    __syncthreads();
    float s = 0.f;
    for (int i = 0; i < (int)(blockDim.x >> 6); ++i) s += sh[i];
    return s;
}
__device__ float block_max(float v, float* sh) {
    for (int o = 32; o >= 1; o >>= 1) v = fmaxf(v, __shfl_xor(v, o));
    int w = threadIdx.x >> 6;
    __syncthreads();
    if ((threadIdx.x & 63) == 0) sh[w] = v;
    __syncthreads();
    float s = -INFINITY;
    for (int i = 0; i < (int)(blockDim.x >> 6); ++i) s = fmaxf(s, sh[i]);
    return s;
}

__device__ void ln512(const float* xin, float* out, float* sh) {
    int t = threadIdx.x;
    float a = xin[t], b = xin[t + 256];
    float m = block_sum(a + b, sh) * (1.0f / D);
    float v = block_sum((a - m) * (a - m) + (b - m) * (b - m), sh) * (1.0f / D);
    m = rb(m); v = rb(v);
    float den = rb(sqrtf(rb(v + 1e-5f)));
    out[t] = rb(rb(a - m) / den);
    out[t + 256] = rb(rb(b - m) / den);
    __syncthreads();
}

__global__ void k_q(const u16* x, const u16* wq, float* q) {
    __shared__ float xs[D], ln[D], sh[8];
    int b = blockIdx.y, t = threadIdx.x;
    xs[t] = bf2f(x[b * D + t]); xs[t + 256] = bf2f(x[b * D + t + 256]);
    __syncthreads();
    ln512(xs, ln, sh);
    int col = blockIdx.x * 256 + t;
    float acc = 0.f;
    for (int i = 0; i < D; ++i) acc += ln[i] * bf2f(wq[(size_t)i * (HQ * DH) + col]);
    q[b * HQ * DH + col] = rb(acc);
}

__global__ void k_attn(const float* q, const u16* kc, const u16* vc, float* o) {
    __shared__ float qs[DH], sc[W], sh[8];
    int h = blockIdx.x, b = blockIdx.y, t = threadIdx.x, kvh = h / (HQ / HKV);
    if (t < DH) qs[t] = q[b * HQ * DH + h * DH + t];
    __syncthreads();
    const u16* kb = kc + ((size_t)(b * HKV + kvh) * SK + (SK - W)) * DH;
    const u16* vb = vc + ((size_t)(b * HKV + kvh) * SK + (SK - W)) * DH;
    float mx = -INFINITY;
    for (int j = t; j < W; j += 256) {
        float acc = 0.f;
        for (int d = 0; d < DH; ++d) acc += qs[d] * bf2f(kb[(size_t)j * DH + d]);
        acc = rb(rb(acc) * 0.08838834764831845f);
        sc[j] = acc; mx = fmaxf(mx, acc);
    }
    mx = block_max(mx, sh);
    float s = 0.f;
    for (int j = t; j < W; j += 256) { float e = expf(sc[j] - mx); sc[j] = e; s += e; }
    s = block_sum(s, sh);
    float inv = 1.0f / s;
    for (int j = t; j < W; j += 256) sc[j] = rb(sc[j] * inv);
    __syncthreads();
    if (t < DH) {
        float acc = 0.f;
        for (int j = 0; j < W; ++j) acc += sc[j] * bf2f(vb[(size_t)j * DH + t]);
        o[b * HQ * DH + h * DH + t] = rb(acc);
    }
}

__global__ void k_o(const u16* x, const float* o, const u16* wo, float* h) {
    int b = blockIdx.y, col = blockIdx.x * 256 + threadIdx.x;
    float acc = 0.f;
    for (int i = 0; i < HQ * DH; ++i) acc += o[b * HQ * DH + i] * bf2f(wo[(size_t)i * D + col]);
    h[b * D + col] = rb(bf2f(x[b * D + col]) + rb(acc));
}

__global__ void k_w1(const float* h, const u16* w1, float* h1) {
    __shared__ float xs[D], ln[D], sh[8];
    int b = blockIdx.y, t = threadIdx.x;
    xs[t] = h[b * D + t]; xs[t + 256] = h[b * D + t + 256];
    __syncthreads();
    ln512(xs, ln, sh);
    int col = blockIdx.x * 256 + t;
    float acc = 0.f;
    for (int i = 0; i < D; ++i) acc += ln[i] * bf2f(w1[(size_t)i * F + col]);
    acc = rb(acc);
    h1[b * F + col] = rb(acc / (1.0f + expf(-acc)));
}

__global__ void k_w2(const float* h, const float* h1, const u16* w2, u16* out) {
    int b = blockIdx.y, col = blockIdx.x * 256 + threadIdx.x;
    float acc = 0.f;
    for (int i = 0; i < F; ++i) acc += h1[b * F + i] * bf2f(w2[(size_t)i * D + col]);
    out[b * D + col] = f2bf(h[b * D + col] + rb(acc));
}

extern "C" void kernel_launch(void* const* d_in, const int* in_sizes, int n_in,
                              void* d_out, int out_size, void* d_ws, size_t ws_size,
                              hipStream_t stream) {
    const u16* x = (const u16*)d_in[0];
    const u16* kc = (const u16*)d_in[1];
    const u16* vc = (const u16*)d_in[2];
    const u16* wq = (const u16*)d_in[3];
    const u16* wo = (const u16*)d_in[4];
    const u16* w1 = (const u16*)d_in[6];
    const u16* w2 = (const u16*)d_in[7];
    float* ws = (float*)d_ws;
    float* q = ws; float* o = ws + 8192; float* h = ws + 16384; float* h1 = ws + 20480;
    k_q<<<dim3(16, B), 256, 0, stream>>>(x, wq, q);
    k_attn<<<dim3(HQ, B), 256, 0, stream>>>(q, kc, vc, o);
    k_o<<<dim3(2, B), 256, 0, stream>>>(x, o, wo, h);
    k_w1<<<dim3(7, B), 256, 0, stream>>>(h, w1, h1);
    k_w2<<<dim3(2, B), 256, 0, stream>>>(h, h1, w2, (u16*)d_out);
}
```

```cpp
#include <hip/hip_runtime.h>
#include <hip/hip_bf16.h>

typedef unsigned short u16;
typedef unsigned long long u64;
typedef unsigned v4u __attribute__((ext_vector_type(4)));
typedef unsigned v2u __attribute__((ext_vector_type(2)));
typedef int v4i __attribute__((ext_vector_type(4)));
typedef float v4f __attribute__((ext_vector_type(4)));
typedef float v2f __attribute__((ext_vector_type(2)));
typedef short v4s __attribute__((ext_vector_type(4)));
typedef __bf16 v8bf __attribute__((ext_vector_type(8)));
typedef __bf16 v2bf __attribute__((ext_vector_type(2)));
#define LAS __attribute__((address_space(3)))
#define DEV __device__ __forceinline__

#ifndef NV1
#define NV1 6
#endif
#define WOD 60
#define NTS " nt"
constexpr int SPIN_MAX = 20000;

constexpr unsigned WS_Q = 4096;
constexpr unsigned WS_ML = WS_Q + 262144;
constexpr unsigned WS_O = WS_ML + 16384;
constexpr unsigned WS_H = WS_O + 262144;
constexpr unsigned WS_Y = WS_H + 32768;
constexpr unsigned WS_END = WS_Y + 458752;

constexpr unsigned L_A = 0;
constexpr unsigned L_B = 65536;
constexpr unsigned L_C = 131072;
constexpr unsigned L_LN = 147456;
constexpr unsigned L_QI = L_LN + 640;
constexpr unsigned L_MX = L_QI + 1280;
constexpr unsigned L_LS = L_MX + 128;
constexpr unsigned L_ZR = L_LS + 128;
constexpr unsigned L_GS = L_ZR + 1024;
constexpr unsigned L_ZE = L_GS + 128;
constexpr unsigned L_END = L_ZE + 2048;

DEV float bf_lo(unsigned v) { return __uint_as_float(v << 16); }
DEV float bf_hi(unsigned v) { return __uint_as_float(v & 0xffff0000u); }
DEV unsigned cvtpk(float a, float b) { v2bf r = {(__bf16)a, (__bf16)b}; return __builtin_bit_cast(unsigned, r); }
DEV float rb(float a) { return bf_lo(cvtpk(a, a)); }
template <int CTRL> DEV float dppf(float x) { return __builtin_bit_cast(float, __builtin_amdgcn_mov_dpp(__builtin_bit_cast(int, x), CTRL, 0xf, 0xf, true)); }
template <int CTRL> DEV unsigned dppu(unsigned x) { return (unsigned)__builtin_amdgcn_mov_dpp((int)x, CTRL, 0xf, 0xf, true); }
constexpr int XOR1 = 0xB1, XOR2 = 0x4E, XOR7 = 0x141, XOR8 = 0x128;
DEV float x16_sum(float x) { auto s = __builtin_amdgcn_permlane16_swap(__float_as_uint(x), __float_as_uint(x), false, false); return __uint_as_float(s[0]) + __uint_as_float(s[1]); }
DEV float x32_sum(float x) { auto s = __builtin_amdgcn_permlane32_swap(__float_as_uint(x), __float_as_uint(x), false, false); return __uint_as_float(s[0]) + __uint_as_float(s[1]); }
DEV float x16_max(float x) { auto s = __builtin_amdgcn_permlane16_swap(__float_as_uint(x), __float_as_uint(x), false, false); return fmaxf(__uint_as_float(s[0]), __uint_as_float(s[1])); }
DEV float x32_max(float x) { auto s = __builtin_amdgcn_permlane32_swap(__float_as_uint(x), __float_as_uint(x), false, false); return fmaxf(__uint_as_float(s[0]), __uint_as_float(s[1])); }
DEV float sum8(float x) { x += dppf<XOR1>(x); x += dppf<XOR2>(x); x += dppf<XOR7>(x); return x; }
DEV float max8(float x) { x = fmaxf(x, dppf<XOR1>(x)); x = fmaxf(x, dppf<XOR2>(x)); x = fmaxf(x, dppf<XOR7>(x)); return x; }
DEV float sum16(float x) { x = sum8(x); x += dppf<XOR8>(x); return x; }
DEV v4f mfma(v4u a, v4u b, v4f c) { return __builtin_amdgcn_mfma_f32_16x16x32_bf16(__builtin_bit_cast(v8bf, a), __builtin_bit_cast(v8bf, b), c, 0, 0, 0); }
DEV v4i mksrd(const void* p, unsigned n) { u64 a = (u64)p; v4i r; r.x = (int)(unsigned)a; r.y = (int)(unsigned)(a >> 32); r.z = (int)n; r.w = 0x00020000; return r; }

template <typename T> DEV T lds_ld(unsigned a) { return *(LAS T*)(size_t)a; }
template <typename T> DEV void lds_st(unsigned a, T v) { *(LAS T*)(size_t)a = v; }
DEV v2u lds_tr(unsigned a) { return __builtin_bit_cast(v2u, __builtin_amdgcn_ds_read_tr16_b64_v4i16((LAS v4s*)(size_t)a)); }
DEV v4u frag_tr(unsigned a0, unsigned a1) { v2u p = lds_tr(a0), q = lds_tr(a1); v4u r = {p.x, p.y, q.x, q.y}; return r; }

DEV void dma(unsigned ldsdst, unsigned voff, v4i srd, unsigned soff) {
    asm volatile("s_mov_b32 m0, %0\n\ts_nop 0\n\tbuffer_load_dwordx4 %1, %2, %3 offen" NTS " lds" ::"s"(ldsdst), "v"(voff), "s"(srd), "s"(soff) : "memory");
}
#define WAITV(n) asm volatile("s_waitcnt vmcnt(%0)" ::"n"(n) : "memory")
#define BAR() asm volatile("s_waitcnt lgkmcnt(0)\n\ts_barrier" ::: "memory")
DEV void put16(v4i srd, unsigned voff, unsigned a, unsigned b, unsigned tag) {
    v4u v = {a, tag, b, tag};
    asm volatile("buffer_store_dwordx4 %0, %1, %2, 0 offen sc1\n\ts_nop 1" ::"v"(v), "v"(voff), "s"(srd) : "memory");
}
DEV bool tag_ok(v4u v, unsigned tag) { return (v.y == tag) & (v.w == tag); }

#define ST(i) do { } while (0)

__global__ void __launch_bounds__(512, 2)
fused_block(const u16* __restrict__ x, const u16* __restrict__ kc, const u16* __restrict__ vc, const u16* __restrict__ wq,
            const u16* __restrict__ wo, const u16* __restrict__ w1, const u16* __restrict__ w2, u16* __restrict__ out,
            unsigned char* __restrict__ ws) {
    __shared__ __attribute__((aligned(1024))) unsigned char lds_raw[L_END];
    const unsigned LB = (unsigned)(size_t)(LAS unsigned char*)lds_raw;
    const int tid = threadIdx.x, l = tid & 63;
    const int w = __builtin_amdgcn_readfirstlane(tid >> 6);
    const int xg = blockIdx.x & 7, u = blockIdx.x >> 3;
    const int gq = l >> 4, c = l & 15;
    const bool att = u >= 16;
    const int b = (u >> 3) & 1, s = u & 7;
    const int kq = u & 3, cq = u >> 2;
    const v4i s_ws = mksrd(ws, WS_END), s_x = mksrd(x, 2048);
    asm volatile("s_nop 4" ::"s"(kc), "s"(vc), "s"(wq), "s"(wo), "s"(w1), "s"(w2), "s"(out) : "memory");

    unsigned ev, xv; v4u xa, xb;
    {
        const unsigned zo = 0, xo = (unsigned)l * 32u, xvo = (unsigned)(((tid >> 7) & 1) * 512 + kq * 128 + (tid & 127)) * 2u;
        asm volatile("buffer_load_dword %0, %4, %5, 0 offen sc1\n\t"
                     "buffer_load_dwordx4 %1, %6, %7, 0 offen\n\t"
                     "buffer_load_dwordx4 %2, %6, %7, 0 offen offset:16\n\t"
                     "buffer_load_ushort %3, %8, %7, 0 offen"
                     : "=&v"(ev), "=&v"(xa), "=&v"(xb), "=&v"(xv) : "v"(zo), "s"(s_ws), "v"(xo), "s"(s_x), "v"(xvo) : "memory");
    }
    const unsigned p8 = (unsigned)(((l & 7) ^ (((l >> 4) & 1) << 1)) * 16);
    {
        const v4i s_wq = mksrd(wq, 4u << 20);
        const unsigned vo = (unsigned)(l >> 3) * 8192u + p8;
        const unsigned so = (unsigned)(kq * 128 + w * 16) * 8192u + (unsigned)(xg * 512 + cq * 64) * 2u;
        dma(LB + L_C + w * 2048, vo, s_wq, so);
        dma(LB + L_C + w * 2048 + 1024, vo ^ 64u, s_wq, so + 8u * 8192u);
    }
    __builtin_amdgcn_s_barrier();
    const v4i s_k = mksrd(kc, 32u << 20), s_v = mksrd(vc, 32u << 20);
    const unsigned kvb = (unsigned)((b * 8 + xg) * 8192 + 6144 + s * 256 + w * 32) * 256u;
    const unsigned kvo = (unsigned)gq * 256u + (unsigned)((c ^ gq) * 16);
    if (att) {
#pragma unroll
        for (int i = 0; i < 6; ++i) dma(LB + L_A + (w * 8 + i) * 1024, kvo ^ (unsigned)((i & 3) * 64), s_k, kvb + i * 1024);
    }
    asm volatile("s_cmp_lg_u32 %4, 0\n\ts_cbranch_scc1 1f\n\ts_waitcnt vmcnt(2)\n1:\n\ts_waitcnt vmcnt(8)"
                 : "+v"(ev), "+v"(xa), "+v"(xb), "+v"(xv) : "s"(u >> 4) : "memory", "scc");
    const unsigned tag = (unsigned)__builtin_amdgcn_readfirstlane((int)ev) + 1u;

    {
        float v[16];
        v[0] = bf_lo(xa.x); v[1] = bf_hi(xa.x); v[2] = bf_lo(xa.y); v[3] = bf_hi(xa.y); v[4] = bf_lo(xa.z); v[5] = bf_hi(xa.z); v[6] = bf_lo(xa.w); v[7] = bf_hi(xa.w);
        v[8] = bf_lo(xb.x); v[9] = bf_hi(xb.x); v[10] = bf_lo(xb.y); v[11] = bf_hi(xb.y); v[12] = bf_lo(xb.z); v[13] = bf_hi(xb.z); v[14] = bf_lo(xb.w); v[15] = bf_hi(xb.w);
        float sm = 0.f;
#pragma unroll
        for (int i = 0; i < 16; ++i) sm += v[i];
        const float mean = x16_sum(sum16(sm)) * (1.0f / 512.0f);
        float sq = 0.f;
#pragma unroll
        for (int i = 0; i < 16; ++i) { const float d = v[i] - mean; sq += d * d; }
        const float var = x16_sum(sum16(sq)) * (1.0f / 512.0f);
        const float mr = rb(mean), den = rb(__fsqrt_rn(rb(rb(var) + 1e-5f)));
        const int src = ((w >> 1) & 1) * 32;
        const float mb = __builtin_bit_cast(float, __builtin_amdgcn_readlane(__builtin_bit_cast(int, mr), src));
        const float db = __builtin_bit_cast(float, __builtin_amdgcn_readlane(__builtin_bit_cast(int, den), src));
        const float ln = __fdiv_rn(rb(bf_lo(xv) - mb), db);
        if (w < 4) lds_st<u16>(LB + L_LN + (tid >> 7) * 320 + (tid & 127) * 2, (u16)cvtpk(ln, ln));
    }
    if (att) WAITV(6); else WAITV(0);
    BAR();
    ST(1);
    const unsigned q4 = (unsigned)((l >> 2) & 3), p4 = (unsigned)(l & 3);
    const unsigned trsw = (q4 >> 1) | ((unsigned)(gq & 1) << 1);
    const unsigned tr128 = (unsigned)(gq * 8 + q4) * 128u + p4 * 8u;
    if (w < 4) {
        const unsigned a = LB + L_C + tr128 + (((unsigned)w ^ trsw) * 32u);
        const unsigned la = LB + L_LN + (c & 1) * 320 + gq * 16;
        v4f acc = {0.f, 0.f, 0.f, 0.f};
#pragma unroll
        for (int ks = 0; ks < 4; ++ks) acc = mfma(frag_tr(a + ks * 4096, a + ks * 4096 + 512), lds_ld<v4u>(la + ks * 64), acc);
        const bool hi = (c >> 1) & 1;
        const float v0 = hi ? acc.z : acc.x, v1 = hi ? acc.w : acc.y;
        const unsigned off = WS_Q + (unsigned)((((xg * 2 + (c & 1)) * 4 + kq) * 512) + cq * 64 + w * 16 + gq * 4 + (hi ? 2 : 0)) * 8u;
        if (c < 4) put16(s_ws, off, __float_as_uint(v0), __float_as_uint(v1), tag);
    }
    __builtin_amdgcn_s_barrier();
    ST(2);

    if (att) {
        const unsigned vvo = (unsigned)gq * 256u + (unsigned)((c ^ (gq << 1)) * 16);
#pragma unroll
        for (int i = 6; i < 8; ++i) dma(LB + L_A + (w * 8 + i) * 1024, kvo ^ (unsigned)((i & 3) * 64), s_k, kvb + i * 1024);
#pragma unroll
        for (int i = 0; i < NV1; ++i) dma(LB + L_B + (w * 8 + i) * 1024, vvo ^ (unsigned)((i & 1) * 128), s_v, kvb + i * 1024);
        WAITV(NV1);
        ST(3);
        v4u g0, g1, g2, g3;
        const unsigned qo = WS_Q + (unsigned)(xg * 2 + b) * 16384u + (unsigned)(tid & 255) * 16u;
        const unsigned qo2 = qo + 8192u;
        if (w < 4) {
            const unsigned k4 = 4096u;
            asm volatile("buffer_load_dwordx4 %0, %4, %6, 0 offen sc1\n\t"
                         "buffer_load_dwordx4 %1, %4, %6, %7 offen sc1\n\t"
                         "buffer_load_dwordx4 %2, %5, %6, 0 offen sc1\n\t"
                         "buffer_load_dwordx4 %3, %5, %6, %7 offen sc1"
                         : "=&v"(g0), "=&v"(g1), "=&v"(g2), "=&v"(g3) : "v"(qo), "v"(qo2), "s"(s_ws), "s"(k4) : "memory");
        }
#pragma unroll
        for (int i = NV1; i < 8; ++i) dma(LB + L_B + (w * 8 + i) * 1024, vvo ^ (unsigned)((i & 1) * 128), s_v, kvb + i * 1024);
        v4u kf[2][4];
#pragma unroll
        for (int T = 0; T < 2; ++T)
#pragma unroll
            for (int st = 0; st < 4; ++st)
                kf[T][st] = lds_ld<v4u>(LB + L_A + (unsigned)(w * 32 + T * 16 + c) * 256u + (unsigned)(((st * 4 + gq) ^ c) * 16));
        if (w < 4) {
            asm volatile("s_waitcnt vmcnt(%4)" : "+v"(g0), "+v"(g1), "+v"(g2), "+v"(g3) : "n"(8 - NV1) : "memory");
            int spins = 0;
            while (!__all(tag_ok(g0, tag) & tag_ok(g1, tag) & tag_ok(g2, tag) & tag_ok(g3, tag)) && ++spins < SPIN_MAX) {
                const unsigned k4 = 4096u;
                asm volatile("buffer_load_dwordx4 %0, %4, %6, 0 offen sc1\n\t"
                             "buffer_load_dwordx4 %1, %4, %6, %7 offen sc1\n\t"
                             "buffer_load_dwordx4 %2, %5, %6, 0 offen sc1\n\t"
                             "buffer_load_dwordx4 %3, %5, %6, %7 offen sc1\n\t"
                             "s_waitcnt vmcnt(0)"
                             : "=&v"(g0), "=&v"(g1), "=&v"(g2), "=&v"(g3) : "v"(qo), "v"(qo2), "s"(s_ws), "s"(k4) : "memory");
            }
            const float qa = (__uint_as_float(g0.x) + __uint_as_float(g1.x)) + (__uint_as_float(g2.x) + __uint_as_float(g3.x));
            const float qb = (__uint_as_float(g0.z) + __uint_as_float(g1.z)) + (__uint_as_float(g2.z) + __uint_as_float(g3.z));
            lds_st<unsigned>(LB + L_QI + w * 320 + l * 4, cvtpk(qa, qb));
        }
        BAR();
        ST(4);
        v4f sc[2] = {{0.f, 0.f, 0.f, 0.f}, {0.f, 0.f, 0.f, 0.f}};
#pragma unroll
        for (int st = 0; st < 4; ++st) {
            const v4u qf = lds_ld<v4u>(LB + L_QI + (c & 3) * 320 + st * 64 + gq * 16);
            sc[0] = mfma(kf[0][st], qf, sc[0]);
            sc[1] = mfma(kf[1][st], qf, sc[1]);
        }
        float sv[8];
#pragma unroll
        for (int i = 0; i < 4; ++i) {
            const float a0 = sc[i >> 1][(i & 1) * 2], a1 = sc[i >> 1][(i & 1) * 2 + 1];
            const unsigned r1 = cvtpk(a0, a1);
            const unsigned r2 = cvtpk(bf_lo(r1) * 0.08838834764831845f, bf_hi(r1) * 0.08838834764831845f);
            sv[2 * i] = bf_lo(r2); sv[2 * i + 1] = bf_hi(r2);
        }
        float mx = fmaxf(fmaxf(fmaxf(sv[0], sv[1]), fmaxf(sv[2], sv[3])), fmaxf(fmaxf(sv[4], sv[5]), fmaxf(sv[6], sv[7])));
        mx = x32_max(x16_max(mx));
        if (l < 4) lds_st<float>(LB + L_MX + l * 32 + w * 4, mx);
        BAR();
        {
            const v4f m0 = lds_ld<v4f>(LB + L_MX + (c & 3) * 32), m1 = lds_ld<v4f>(LB + L_MX + (c & 3) * 32 + 16);
            mx = fmaxf(fmaxf(fmaxf(m0.x, m0.y), fmaxf(m0.z, m0.w)), fmaxf(fmaxf(m1.x, m1.y), fmaxf(m1.z, m1.w)));
        }
        float e[8], ls = 0.f;
#pragma unroll
        for (int i = 0; i < 8; ++i) { e[i] = __expf(sv[i] - mx); ls += e[i]; }
        ls = x32_sum(x16_sum(ls));
        if (l < 4) lds_st<float>(LB + L_LS + l * 32 + w * 4, ls);
        const v4u pf = {cvtpk(e[0], e[1]), cvtpk(e[2], e[3]), cvtpk(e[4], e[5]), cvtpk(e[6], e[7])};
        v4u pm[4];
#pragma unroll
        for (int xx = 0; xx < 4; ++xx) { const unsigned m = ((c >> 2) == xx) ? 0xffffffffu : 0u; pm[xx] = pf & m; }
        BAR();
        if (w == 7) {
            const v4f a0 = lds_ld<v4f>(LB + L_LS + (c & 3) * 32), a1 = lds_ld<v4f>(LB + L_LS + (c & 3) * 32 + 16);
            const float lt = ((a0.x + a0.y) + (a0.z + a0.w)) + ((a1.x + a1.y) + (a1.z + a1.w));
            if (l < 4) put16(s_ws, WS_ML + (unsigned)((b * 8 + xg) * 8 + s) * 128u + (unsigned)l * 16u, __float_as_uint(mx), __float_as_uint(lt), tag);
            WAITV(1);
        } else {
            WAITV(0);
        }
        ST(5);
        {
            const unsigned row = (unsigned)(w * 32 + gq * 4) + q4;
            const unsigned vb = LB + L_B + row * 256u + p4 * 8u;
            const unsigned sw = row & 7u;
            v4f oa = {0.f, 0.f, 0.f, 0.f}, ob = {0.f, 0.f, 0.f, 0.f};
#pragma unroll
            for (int xx = 0; xx < 4; ++xx) {
                const unsigned a0 = vb + (((unsigned)xx ^ sw) * 32u), a1 = vb + (((unsigned)(xx + 4) ^ sw) * 32u);
                oa = mfma(frag_tr(a0, a0 + 4096), pm[xx], oa);
                ob = mfma(frag_tr(a1, a1 + 4096), pm[xx], ob);
            }
            const unsigned ra = LB + L_C + (unsigned)w * 2048u + (unsigned)((c & 3) * 128 + (c >> 2) * 16 + gq * 4) * 4u;
            lds_st<v4f>(ra, oa);
            lds_st<v4f>(ra + 256, ob);
        }
        BAR();
        if (w < 4) {
            const unsigned un = (unsigned)(w * 32 + (l & 31));
            const unsigned ra = LB + L_C + (unsigned)(l >> 5) * 8192u + un * 16u;
            const v4f a0 = lds_ld<v4f>(ra), a1 = lds_ld<v4f>(ra + 2048), a2 = lds_ld<v4f>(ra + 4096), a3 = lds_ld<v4f>(ra + 6144);
            const v4f t = (a0 + a1) + (a2 + a3);
            const float o0 = x32_sum(t.x), o1 = x32_sum(t.y), o2 = x32_sum(t.z), o3 = x32_sum(t.w);
            const unsigned off = WS_O + (unsigned)(b * 8 + xg) * 16384u + (un >> 4) * 2048u + (unsigned)s * 256u + (un & 15u) * 16u;
            if (l < 32) put16(s_ws, off, cvtpk(o0, o1), cvtpk(o2, o3), tag);
        }
        ST(6);
        if (s == 7) goto done;
        __builtin_amdgcn_s_barrier();
        {
            const int fb = xg * 7 + s;
            const v4i s_w1 = mksrd(w1, 1835008u), s_w2 = mksrd(w2, 1835008u);
            {
                const unsigned vo = (unsigned)(l >> 2) * 3584u + (unsigned)(((l & 3) ^ ((l >> 5) << 1)) * 16);
                const unsigned so = (unsigned)(w * 64) * 3584u + (unsigned)fb * 64u;
#pragma unroll
                for (int i = 0; i < 4; ++i) dma(LB + L_A + (w * 4 + i) * 1024, vo, s_w1, so + i * 16 * 3584);
                const unsigned vo2 = (unsigned)(l >> 3) * 1024u + p8;
                const unsigned so2 = (unsigned)(fb * 32) * 1024u + (unsigned)w * 128u;
#pragma unroll
                for (int i = 0; i < 4; ++i) dma(LB + L_A + 32768 + (w * 4 + i) * 1024, vo2 ^ (unsigned)((i & 1) * 64), s_w2, so2 + i * 8192);
            }
            const unsigned hs = LB + L_B + (unsigned)w * 4096u;
            {
                unsigned xr;
                const unsigned xo = (unsigned)(b * 512 + w * 64 + (l & 31) * 2) * 2u;
                asm volatile("buffer_load_dword %0, %1, %2, 0 offen\n\ts_waitcnt vmcnt(0)" : "=&v"(xr) : "v"(xo), "s"(s_x) : "memory");
                const v4u z4 = {0u, 0u, 0u, 0u};
                lds_st<v4u>(hs + l * 16, z4); lds_st<v4u>(hs + 1024 + l * 16, z4); lds_st<v4u>(hs + 2048 + l * 16, z4);
                lds_st<unsigned>(hs + (l < 32 ? 8 : 12) * 160 + (l & 31) * 4, l < 32 ? xr : 0x3f803f80u);
            }
            v4u f1[2][2], f2[4];
            {
                const unsigned a = LB + L_A + (unsigned)(w * 64 + gq * 8 + q4) * 64u + p4 * 8u;
                const unsigned sx = (unsigned)(gq & 1) * 32u;
#pragma unroll
                for (int ks = 0; ks < 2; ++ks) {
                    f1[ks][0] = frag_tr(a + sx + ks * 2048, a + sx + ks * 2048 + 256);
                    f1[ks][1] = frag_tr(a + (sx ^ 32u) + ks * 2048, a + (sx ^ 32u) + ks * 2048 + 256);
                }
                const unsigned a2 = LB + L_A + 32768 + (unsigned)w * 4096u + tr128;
#pragma unroll
                for (int nt = 0; nt < 4; ++nt) { const unsigned q = a2 + (((unsigned)nt ^ trsw) * 32u); f2[nt] = frag_tr(q, q + 512); }
            }
            const unsigned ha = hs + (unsigned)c * 160u + (unsigned)gq * 16u;
            float xw, cs;
            {
                v4f d0 = {0.f, 0.f, 0.f, 0.f}, d1 = {0.f, 0.f, 0.f, 0.f};
#pragma unroll
                for (int ks = 0; ks < 2; ++ks) { const v4u xf = lds_ld<v4u>(ha + ks * 64); d0 = mfma(xf, f1[ks][0], d0); d1 = mfma(xf, f1[ks][1], d1); }
                if (gq >= 2) { const unsigned a = LB + L_ZE + (unsigned)(w * 2 + (gq & 1)) * 128u + (unsigned)c * 4u; lds_st<float>(a, d0.x); lds_st<float>(a + 64, d1.x); }
                BAR();
                const unsigned a = LB + L_ZE + (unsigned)(c >> 3) * 1024u + (unsigned)(gq * 8 + (c & 7)) * 4u;
                xw = (lds_ld<float>(a) + lds_ld<float>(a + 256)) + (lds_ld<float>(a + 512) + lds_ld<float>(a + 768));
                cs = (lds_ld<float>(a + 128) + lds_ld<float>(a + 384)) + (lds_ld<float>(a + 640) + lds_ld<float>(a + 896));
                xw += dppf<XOR8>(xw); cs += dppf<XOR8>(cs);
            }
            ST(7);
            {
                v4u h0, h1;
                const unsigned ho = WS_H + (unsigned)(b * 8 + w) * 2048u + (unsigned)l * 16u;
                int spins = 0;
                do {
                    asm volatile("buffer_load_dwordx4 %0, %2, %3, 0 offen sc1\n\t"
                                 "buffer_load_dwordx4 %1, %2, %3, 0 offen offset:1024 sc1\n\t"
                                 "s_waitcnt vmcnt(0)"
                                 : "=&v"(h0), "=&v"(h1) : "v"(ho), "s"(s_ws) : "memory");
                } while (!__all(tag_ok(h0, tag) & tag_ok(h1, tag)) && ++spins < SPIN_MAX);
                const v2u a0 = {h0.x, h0.z}, a1 = {h1.x, h1.z};
                lds_st<v2u>(hs + (unsigned)gq * 160u + (unsigned)c * 8u, a0);
                lds_st<v2u>(hs + (unsigned)(gq + 4) * 160u + (unsigned)c * 8u, a1);
            }
            ST(8);
            {
                v4f d0 = {0.f, 0.f, 0.f, 0.f}, d1 = {0.f, 0.f, 0.f, 0.f}, gm = {0.f, 0.f, 0.f, 0.f};
#pragma unroll
                for (int ks = 0; ks < 2; ++ks) {
                    const v4u xf = lds_ld<v4u>(ha + ks * 64);
                    gm = mfma(xf, xf, gm); d0 = mfma(xf, f1[ks][0], d0); d1 = mfma(xf, f1[ks][1], d1);
                }
                float gs = (gm.x + gm.y) + (gm.z + gm.w);
                gs = sum16(c == 12 ? 0.f : gs);
                const float z0 = x16_sum((d0.x + d0.y) + (d0.z + d0.w)), z1 = x16_sum((d1.x + d1.y) + (d1.z + d1.w));
                if (c == 0) lds_st<float>(LB + L_GS + w * 16 + gq * 4, gs);
                if (l < 16) { lds_st<float>(LB + L_ZR + w * 128 + l * 4, z0); lds_st<float>(LB + L_ZR + w * 128 + 64 + l * 4, z1); }
            }
            BAR();
            {
                const v4f g4 = lds_ld<v4f>(LB + L_GS + (l & 7) * 16);
                const unsigned a = LB + L_ZR + (unsigned)(c >> 3) * 512u + (unsigned)(gq * 8 + (c & 7)) * 4u;
                float z = (lds_ld<float>(a) + lds_ld<float>(a + 128)) + (lds_ld<float>(a + 256) + lds_ld<float>(a + 384));
                const float s2 = sum8((g4.x + g4.y) + g4.z) * (1.0f / 512.0f), mean = sum8(g4.w) * (1.0f / 512.0f);
                z += dppf<XOR8>(z);
                const float var = fmaxf(s2 - mean * mean, 0.f);
                const float mr = rb(mean), den = rb(__fsqrt_rn(rb(rb(var) + 1e-5f)));
                z = rb(((z + xw) - mr * cs) * __builtin_amdgcn_rcpf(den));
                const float hv = z * __builtin_amdgcn_rcpf(1.0f + __expf(-z));
                const unsigned pk = cvtpk(hv, dppf<0x101>(hv));
                const v4u af = {pk, dppu<0x102>(pk), dppu<0x104>(pk), dppu<0x106>(pk)};
                const v4f zz = {0.f, 0.f, 0.f, 0.f};
                const v4f y0 = mfma(af, f2[0], zz), y1 = mfma(af, f2[1], zz), y2 = mfma(af, f2[2], zz), y3 = mfma(af, f2[3], zz);
                const bool hi = gq >= 2;
                const float ya = hi ? y2.x : y0.x, yb = hi ? y3.x : y1.x;
                const unsigned rblk = (unsigned)(b * 32 + w * 4 + (gq >> 1) * 2 + (c >> 3));
                const unsigned off = WS_Y + (rblk * 56u + (unsigned)fb) * 128u + (unsigned)(c & 7) * 16u;
                if ((gq & 1) == 0) put16(s_ws, off, __float_as_uint(ya), __float_as_uint(yb), tag);
            }
            ST(9);
        }
    } else {
        if (WOD) __builtin_amdgcn_s_sleep(WOD);
        {
            const v4i s_wo = mksrd(wo, 4u << 20);
            const unsigned vo = (unsigned)(l >> 3) * 1024u + p8;
            const unsigned so = (unsigned)(xg * 512 + w * 64) * 1024u + (unsigned)s * 128u;
#pragma unroll
            for (int i = 0; i < 8; ++i) dma(LB + L_A + (w * 8 + i) * 1024, vo ^ (unsigned)((i & 1) * 64), s_wo, so + i * 8192);
        }
        WAITV(0);
        BAR();
        ST(3);
        v4u wf[16];
        float wgt[4];
        if (w < 4) {
            const unsigned a = LB + L_A + tr128 + (((unsigned)w ^ trsw) * 32u);
#pragma unroll
            for (int ks = 0; ks < 16; ++ks) wf[ks] = frag_tr(a + ks * 4096, a + ks * 4096 + 512);
            v4u ml;
            const unsigned mo = WS_ML + (unsigned)((b * 8 + xg) * 8 + (c & 7)) * 128u + (unsigned)gq * 16u;
            int spins = 0;
            do {
                asm volatile("buffer_load_dwordx4 %0, %1, %2, 0 offen sc1\n\ts_waitcnt vmcnt(0)" : "=&v"(ml) : "v"(mo), "s"(s_ws) : "memory");
            } while (!__all(tag_ok(ml, tag)) && ++spins < SPIN_MAX);
            const float m = __uint_as_float(ml.x), M = max8(m);
            const float f = __expf(m - M);
            const float L = sum8(f * __uint_as_float(ml.z));
            const float wv = f / L;
#pragma unroll
            for (int h = 0; h < 4; ++h) wgt[h] = __builtin_bit_cast(float, __builtin_amdgcn_ds_bpermute((h * 16 + c) * 4, __builtin_bit_cast(int, wv)));
        }
        BAR();
        ST(4);
        {
            v4u o0, o1;
            const unsigned oo = WS_O + (unsigned)(b * 8 + xg) * 16384u + (unsigned)w * 2048u + (unsigned)l * 16u;
            int spins = 0;
            do {
                asm volatile("buffer_load_dwordx4 %0, %2, %3, 0 offen sc1\n\t"
                             "buffer_load_dwordx4 %1, %2, %3, 0 offen offset:1024 sc1\n\t"
                             "s_waitcnt vmcnt(0)"
                             : "=&v"(o0), "=&v"(o1) : "v"(oo), "s"(s_ws) : "memory");
            } while (!__all(tag_ok(o0, tag) & tag_ok(o1, tag)) && ++spins < SPIN_MAX);
            const v2u a0 = {o0.x, o0.z}, a1 = {o1.x, o1.z};
            const unsigned sa = LB + L_B + (unsigned)gq * 1056u + (unsigned)(w * 64 + c * 4) * 2u;
            lds_st<v2u>(sa, a0);
            lds_st<v2u>(sa + 4 * 1056, a1);
        }
        BAR();
        ST(5);
        if (w < 4) {
            const unsigned oa = LB + L_B + (unsigned)(c & 7) * 1056u + (unsigned)gq * 16u;
            v4u of[16];
#pragma unroll
            for (int k = 0; k < 16; ++k) of[(k & 3) * 4 + (k >> 2)] = lds_ld<v4u>(oa + ((k & 3) * 4 + (k >> 2)) * 64);
            __builtin_amdgcn_sched_barrier(0);
            v4f acc[4] = {{0.f, 0.f, 0.f, 0.f}, {0.f, 0.f, 0.f, 0.f}, {0.f, 0.f, 0.f, 0.f}, {0.f, 0.f, 0.f, 0.f}};
#pragma unroll
            for (int k = 0; k < 4; ++k)
#pragma unroll
                for (int h = 0; h < 4; ++h) acc[h] = mfma(wf[h * 4 + k], of[h * 4 + k], acc[h]);
            __builtin_amdgcn_sched_barrier(0);
            const v4f t = (acc[0] * wgt[0] + acc[1] * wgt[1]) + (acc[2] * wgt[2] + acc[3] * wgt[3]);
            const float r0 = sum8(t.x), r1 = sum8(t.y), r2 = sum8(t.z), r3 = sum8(t.w);
            const unsigned off = WS_H + (unsigned)((b * 8 + s) * 8 + xg) * 256u + (unsigned)(w * 4 + gq) * 16u;
            if (c == 0) put16(s_ws, off, cvtpk(r0, r1), cvtpk(r2, r3), tag);
            ST(6);
        } else if (w == 4 && s < 4) {
            const int e = s >> 1, nh = s & 1, n7 = l & 7;
            const int col = xg * 64 + e * 32 + nh * 8 + n7;
            unsigned xr0, xr1; v4u h0, h1;
            const unsigned xo = (unsigned)(b * 512 + col) * 2u;
            const unsigned ho = WS_H + (unsigned)((b * 8 + xg) * 8 + (l >> 3)) * 256u + (unsigned)(e * 8 + nh * 2 + (n7 >> 2)) * 16u;
            asm volatile("buffer_load_ushort %0, %2, %3, 0 offen\n\tbuffer_load_ushort %1, %2, %3, 0 offen offset:32\n\ts_waitcnt vmcnt(0)"
                         : "=&v"(xr0), "=&v"(xr1) : "v"(xo), "s"(s_x) : "memory");
            int spins = 0;
            do {
                asm volatile("buffer_load_dwordx4 %0, %2, %3, 0 offen sc1\n\t"
                             "buffer_load_dwordx4 %1, %2, %3, 0 offen offset:64 sc1\n\t"
                             "s_waitcnt vmcnt(0)"
                             : "=&v"(h0), "=&v"(h1) : "v"(ho), "s"(s_ws) : "memory");
            } while (!__all(tag_ok(h0, tag) & tag_ok(h1, tag)) && ++spins < SPIN_MAX);
            const unsigned d0 = (n7 & 2) ? h0.z : h0.x, d1 = (n7 & 2) ? h1.z : h1.x;
            float a0 = (n7 & 1) ? bf_hi(d0) : bf_lo(d0), a1 = (n7 & 1) ? bf_hi(d1) : bf_lo(d1);
            a0 += dppf<XOR8>(a0); a1 += dppf<XOR8>(a1);
            a0 = x32_sum(x16_sum(a0)); a1 = x32_sum(x16_sum(a1));
            const float hh0 = rb(bf_lo(xr0) + rb(a0)), hh1 = rb(bf_lo(xr1) + rb(a1));
            ST(7);
            v4u y[7];
            const unsigned yo = WS_Y + (unsigned)(b * 32 + xg * 4 + s) * 7168u + (unsigned)l * 16u, yo2 = yo + 4096u;
            spins = 0;
            bool ok;
            do {
                asm volatile("buffer_load_dwordx4 %0, %7, %9, 0 offen sc1\n\t"
                             "buffer_load_dwordx4 %1, %7, %9, 0 offen offset:1024 sc1\n\t"
                             "buffer_load_dwordx4 %2, %7, %9, 0 offen offset:2048 sc1\n\t"
                             "buffer_load_dwordx4 %3, %7, %9, 0 offen offset:3072 sc1\n\t"
                             "buffer_load_dwordx4 %4, %8, %9, 0 offen sc1\n\t"
                             "buffer_load_dwordx4 %5, %8, %9, 0 offen offset:1024 sc1\n\t"
                             "buffer_load_dwordx4 %6, %8, %9, 0 offen offset:2048 sc1\n\t"
                             "s_waitcnt vmcnt(0)"
                             : "=&v"(y[0]), "=&v"(y[1]), "=&v"(y[2]), "=&v"(y[3]), "=&v"(y[4]), "=&v"(y[5]), "=&v"(y[6])
                             : "v"(yo), "v"(yo2), "s"(s_ws) : "memory");
                ok = true;
#pragma unroll
                for (int k = 0; k < 7; ++k) ok &= tag_ok(y[k], tag);
            } while (!__all(ok) && ++spins < SPIN_MAX);
            ST(8);
            float ya = 0.f, yb = 0.f;
#pragma unroll
            for (int k = 0; k < 7; ++k) { ya += __uint_as_float(y[k].x); yb += __uint_as_float(y[k].z); }
            ya += dppf<XOR8>(ya); yb += dppf<XOR8>(yb);
            ya = x32_sum(x16_sum(ya)); yb = x32_sum(x16_sum(yb));
            const unsigned r = cvtpk(hh0 + rb(ya), hh1 + rb(yb));
            if (l < 8) { out[b * 512 + col] = (u16)r; out[b * 512 + col + 16] = (u16)(r >> 16); }
            if (blockIdx.x == 0 && l == 0) {
                const unsigned zo = 0;
                asm volatile("buffer_store_dword %0, %1, %2, 0 offen sc1" ::"v"(tag), "v"(zo), "s"(s_ws) : "memory");
            }
            ST(9);
        }
    }
done:;
}

extern "C" void kernel_launch(void* const* d_in, const int* in_sizes, int n_in, void* d_out, int out_size, void* d_ws, size_t ws_size,
                              hipStream_t stream) {
    fused_block<<<256, 512, 0, stream>>>((const u16*)d_in[0], (const u16*)d_in[1], (const u16*)d_in[2], (const u16*)d_in[3], (const u16*)d_in[4],
                                         (const u16*)d_in[6], (const u16*)d_in[7], (u16*)d_out, (unsigned char*)d_ws);
}
```

```cpp
#include <hip/hip_runtime.h>
#include <hip/hip_bf16.h>

typedef unsigned short u16;
typedef unsigned long long u64;
typedef unsigned v4u __attribute__((ext_vector_type(4)));
typedef unsigned v2u __attribute__((ext_vector_type(2)));
typedef int v4i __attribute__((ext_vector_type(4)));
typedef float v4f __attribute__((ext_vector_type(4)));
typedef float v2f __attribute__((ext_vector_type(2)));
typedef short v4s __attribute__((ext_vector_type(4)));
typedef __bf16 v8bf __attribute__((ext_vector_type(8)));
typedef __bf16 v2bf __attribute__((ext_vector_type(2)));
#define LAS __attribute__((address_space(3)))
#define DEV __device__ __forceinline__

#ifndef NV1
#define NV1 6
#endif
#define WOD 60
#define NTS " nt"
constexpr int SPIN_MAX = 20000;

constexpr unsigned WS_Q = 4096;
constexpr unsigned WS_ML = WS_Q + 262144;
constexpr unsigned WS_O = WS_ML + 16384;
constexpr unsigned WS_H = WS_O + 262144;
constexpr unsigned WS_Y = WS_H + 32768;
constexpr unsigned WS_END = WS_Y + 458752;

constexpr unsigned L_A = 0;
constexpr unsigned L_B = 65536;
constexpr unsigned L_C = 131072;
constexpr unsigned L_LN = 147456;
constexpr unsigned L_QI = L_LN + 640;
constexpr unsigned L_MX = L_QI + 1280;
constexpr unsigned L_LS = L_MX + 128;
constexpr unsigned L_ZR = L_LS + 128;
constexpr unsigned L_GS = L_ZR + 1024;
constexpr unsigned L_ZE = L_GS + 128;
constexpr unsigned L_END = L_ZE + 2048;

DEV float bf_lo(unsigned v) { return __uint_as_float(v << 16); }
DEV float bf_hi(unsigned v) { return __uint_as_float(v & 0xffff0000u); }
DEV unsigned cvtpk(float a, float b) { v2bf r = {(__bf16)a, (__bf16)b}; return __builtin_bit_cast(unsigned, r); }
DEV float rb(float a) { return bf_lo(cvtpk(a, a)); }
template <int CTRL> DEV float dppf(float x) { return __builtin_bit_cast(float, __builtin_amdgcn_mov_dpp(__builtin_bit_cast(int, x), CTRL, 0xf, 0xf, true)); }
template <int CTRL> DEV unsigned dppu(unsigned x) { return (unsigned)__builtin_amdgcn_mov_dpp((int)x, CTRL, 0xf, 0xf, true); }
constexpr int XOR1 = 0xB1, XOR2 = 0x4E, XOR7 = 0x141, XOR8 = 0x128;
DEV float x16_sum(float x) { auto s = __builtin_amdgcn_permlane16_swap(__float_as_uint(x), __float_as_uint(x), false, false); return __uint_as_float(s[0]) + __uint_as_float(s[1]); }
DEV float x32_sum(float x) { auto s = __builtin_amdgcn_permlane32_swap(__float_as_uint(x), __float_as_uint(x), false, false); return __uint_as_float(s[0]) + __uint_as_float(s[1]); }
DEV float x16_max(float x) { auto s = __builtin_amdgcn_permlane16_swap(__float_as_uint(x), __float_as_uint(x), false, false); return fmaxf(__uint_as_float(s[0]), __uint_as_float(s[1])); }
DEV float x32_max(float x) { auto s = __builtin_amdgcn_permlane32_swap(__float_as_uint(x), __float_as_uint(x), false, false); return fmaxf(__uint_as_float(s[0]), __uint_as_float(s[1])); }
DEV float sum8(float x) { x += dppf<XOR1>(x); x += dppf<XOR2>(x); x += dppf<XOR7>(x); return x; }
DEV float max8(float x) { x = fmaxf(x, dppf<XOR1>(x)); x = fmaxf(x, dppf<XOR2>(x)); x = fmaxf(x, dppf<XOR7>(x)); return x; }
DEV float sum16(float x) { x = sum8(x); x += dppf<XOR8>(x); return x; }
DEV v4f mfma(v4u a, v4u b, v4f c) { return __builtin_amdgcn_mfma_f32_16x16x32_bf16(__builtin_bit_cast(v8bf, a), __builtin_bit_cast(v8bf, b), c, 0, 0, 0); }
DEV v4i mksrd(const void* p, unsigned n) { u64 a = (u64)p; v4i r; r.x = (int)(unsigned)a; r.y = (int)(unsigned)(a >> 32); r.z = (int)n; r.w = 0x00020000; return r; }

template <typename T> DEV T lds_ld(unsigned a) { return *(LAS T*)(size_t)a; }
template <typename T> DEV void lds_st(unsigned a, T v) { *(LAS T*)(size_t)a = v; }
DEV v2u lds_tr(unsigned a) { return __builtin_bit_cast(v2u, __builtin_amdgcn_ds_read_tr16_b64_v4i16((LAS v4s*)(size_t)a)); }
DEV v4u frag_tr(unsigned a0, unsigned a1) { v2u p = lds_tr(a0), q = lds_tr(a1); v4u r = {p.x, p.y, q.x, q.y}; return r; }

template <int NT = 1> DEV void dma(unsigned ldsdst, unsigned voff, v4i srd, unsigned soff) {
    if (NT) asm volatile("s_mov_b32 m0, %0\n\ts_nop 0\n\tbuffer_load_dwordx4 %1, %2, %3 offen nt lds" ::"s"(ldsdst), "v"(voff), "s"(srd), "s"(soff) : "memory");
    else asm volatile("s_mov_b32 m0, %0\n\ts_nop 0\n\tbuffer_load_dwordx4 %1, %2, %3 offen lds" ::"s"(ldsdst), "v"(voff), "s"(srd), "s"(soff) : "memory");
}
#define NT1 1
#define NT2 1
#define NTO 1
#define WAITV(n) asm volatile("s_waitcnt vmcnt(%0)" ::"n"(n) : "memory")
#define BAR() asm volatile("s_waitcnt lgkmcnt(0)\n\ts_barrier" ::: "memory")
DEV void put16(v4i srd, unsigned voff, unsigned a, unsigned b, unsigned tag) {
    v4u v = {a, tag, b, tag};
    asm volatile("buffer_store_dwordx4 %0, %1, %2, 0 offen sc1\n\ts_nop 1" ::"v"(v), "v"(voff), "s"(srd) : "memory");
}
DEV bool tag_ok(v4u v, unsigned tag) { return (v.y == tag) & (v.w == tag); }

#define ST(i) do { } while (0)

__global__ void __launch_bounds__(512, 2)
fused_block(const u16* __restrict__ x, const u16* __restrict__ kc, const u16* __restrict__ vc, const u16* __restrict__ wq,
            const u16* __restrict__ wo, const u16* __restrict__ w1, const u16* __restrict__ w2, u16* __restrict__ out,
            unsigned char* __restrict__ ws) {
    __shared__ __attribute__((aligned(1024))) unsigned char lds_raw[L_END];
    const unsigned LB = (unsigned)(size_t)(LAS unsigned char*)lds_raw;
    const int tid = threadIdx.x, l = tid & 63;
    const int w = __builtin_amdgcn_readfirstlane(tid >> 6);
    const int xg = blockIdx.x & 7, u = blockIdx.x >> 3;
    const int gq = l >> 4, c = l & 15;
    const bool att = u >= 16;
    const int b = (u >> 3) & 1, s = u & 7;
    const int kq = u & 3, cq = u >> 2;
    const v4i s_ws = mksrd(ws, WS_END), s_x = mksrd(x, 2048);
    asm volatile("s_nop 4" ::"s"(kc), "s"(vc), "s"(wq), "s"(wo), "s"(w1), "s"(w2), "s"(out) : "memory");

    unsigned ev, xv; v4u xa, xb;
    {
        const unsigned zo = 0, xo = (unsigned)l * 32u, xvo = (unsigned)(((tid >> 7) & 1) * 512 + kq * 128 + (tid & 127)) * 2u;
        asm volatile("buffer_load_dword %0, %4, %5, 0 offen sc1\n\t"
                     "buffer_load_dwordx4 %1, %6, %7, 0 offen\n\t"
                     "buffer_load_dwordx4 %2, %6, %7, 0 offen offset:16\n\t"
                     "buffer_load_ushort %3, %8, %7, 0 offen"
                     : "=&v"(ev), "=&v"(xa), "=&v"(xb), "=&v"(xv) : "v"(zo), "s"(s_ws), "v"(xo), "s"(s_x), "v"(xvo) : "memory");
    }
    const unsigned p8 = (unsigned)(((l & 7) ^ (((l >> 4) & 1) << 1)) * 16);
    {
        const v4i s_wq = mksrd(wq, 4u << 20);
        const unsigned vo = (unsigned)(l >> 3) * 8192u + p8;
        const unsigned so = (unsigned)(kq * 128 + w * 16) * 8192u + (unsigned)(xg * 512 + cq * 64) * 2u;
        dma(LB + L_C + w * 2048, vo, s_wq, so);
        dma(LB + L_C + w * 2048 + 1024, vo ^ 64u, s_wq, so + 8u * 8192u);
    }
    __builtin_amdgcn_s_barrier();
    const v4i s_k = mksrd(kc, 32u << 20), s_v = mksrd(vc, 32u << 20);
    const unsigned kvb = (unsigned)((b * 8 + xg) * 8192 + 6144 + s * 256 + w * 32) * 256u;
    const unsigned kvo = (unsigned)gq * 256u + (unsigned)((c ^ gq) * 16);
    if (att) {
#pragma unroll
        for (int i = 0; i < 6; ++i) dma(LB + L_A + (w * 8 + i) * 1024, kvo ^ (unsigned)((i & 3) * 64), s_k, kvb + i * 1024);
    }
    asm volatile("s_cmp_lg_u32 %4, 0\n\ts_cbranch_scc1 1f\n\ts_waitcnt vmcnt(2)\n1:\n\ts_waitcnt vmcnt(8)"
                 : "+v"(ev), "+v"(xa), "+v"(xb), "+v"(xv) : "s"(u >> 4) : "memory", "scc");
    const unsigned tag = (unsigned)__builtin_amdgcn_readfirstlane((int)ev) + 1u;

    {
        float v[16];
        v[0] = bf_lo(xa.x); v[1] = bf_hi(xa.x); v[2] = bf_lo(xa.y); v[3] = bf_hi(xa.y); v[4] = bf_lo(xa.z); v[5] = bf_hi(xa.z); v[6] = bf_lo(xa.w); v[7] = bf_hi(xa.w);
        v[8] = bf_lo(xb.x); v[9] = bf_hi(xb.x); v[10] = bf_lo(xb.y); v[11] = bf_hi(xb.y); v[12] = bf_lo(xb.z); v[13] = bf_hi(xb.z); v[14] = bf_lo(xb.w); v[15] = bf_hi(xb.w);
        float sm = 0.f;
#pragma unroll
        for (int i = 0; i < 16; ++i) sm += v[i];
        const float mean = x16_sum(sum16(sm)) * (1.0f / 512.0f);
        float sq = 0.f;
#pragma unroll
        for (int i = 0; i < 16; ++i) { const float d = v[i] - mean; sq += d * d; }
        const float var = x16_sum(sum16(sq)) * (1.0f / 512.0f);
        const float mr = rb(mean), den = rb(__fsqrt_rn(rb(rb(var) + 1e-5f)));
        const int src = ((w >> 1) & 1) * 32;
        const float mb = __builtin_bit_cast(float, __builtin_amdgcn_readlane(__builtin_bit_cast(int, mr), src));
        const float db = __builtin_bit_cast(float, __builtin_amdgcn_readlane(__builtin_bit_cast(int, den), src));
        const float ln = __fdiv_rn(rb(bf_lo(xv) - mb), db);
        if (w < 4) lds_st<u16>(LB + L_LN + (tid >> 7) * 320 + (tid & 127) * 2, (u16)cvtpk(ln, ln));
    }
    if (att) WAITV(6); else WAITV(0);
    BAR();
    ST(1);
    const unsigned q4 = (unsigned)((l >> 2) & 3), p4 = (unsigned)(l & 3);
    const unsigned trsw = (q4 >> 1) | ((unsigned)(gq & 1) << 1);
    const unsigned tr128 = (unsigned)(gq * 8 + q4) * 128u + p4 * 8u;
    if (w < 4) {
        const unsigned a = LB + L_C + tr128 + (((unsigned)w ^ trsw) * 32u);
        const unsigned la = LB + L_LN + (c & 1) * 320 + gq * 16;
        v4f acc = {0.f, 0.f, 0.f, 0.f};
#pragma unroll
        for (int ks = 0; ks < 4; ++ks) acc = mfma(frag_tr(a + ks * 4096, a + ks * 4096 + 512), lds_ld<v4u>(la + ks * 64), acc);
        const bool hi = (c >> 1) & 1;
        const float v0 = hi ? acc.z : acc.x, v1 = hi ? acc.w : acc.y;
        const unsigned off = WS_Q + (unsigned)((((xg * 2 + (c & 1)) * 4 + kq) * 512) + cq * 64 + w * 16 + gq * 4 + (hi ? 2 : 0)) * 8u;
        if (c < 4) put16(s_ws, off, __float_as_uint(v0), __float_as_uint(v1), tag);
    }
    __builtin_amdgcn_s_barrier();
    ST(2);

    if (att) {
        const unsigned vvo = (unsigned)gq * 256u + (unsigned)((c ^ (gq << 1)) * 16);
#pragma unroll
        for (int i = 6; i < 8; ++i) dma(LB + L_A + (w * 8 + i) * 1024, kvo ^ (unsigned)((i & 3) * 64), s_k, kvb + i * 1024);
#pragma unroll
        for (int i = 0; i < NV1; ++i) dma(LB + L_B + (w * 8 + i) * 1024, vvo ^ (unsigned)((i & 1) * 128), s_v, kvb + i * 1024);
        WAITV(NV1);
        ST(3);
        v4u g0, g1, g2, g3;
        const unsigned qo = WS_Q + (unsigned)(xg * 2 + b) * 16384u + (unsigned)(tid & 255) * 16u;
        const unsigned qo2 = qo + 8192u;
        if (w < 4) {
            const unsigned k4 = 4096u;
            asm volatile("buffer_load_dwordx4 %0, %4, %6, 0 offen sc1\n\t"
                         "buffer_load_dwordx4 %1, %4, %6, %7 offen sc1\n\t"
                         "buffer_load_dwordx4 %2, %5, %6, 0 offen sc1\n\t"
                         "buffer_load_dwordx4 %3, %5, %6, %7 offen sc1"
                         : "=&v"(g0), "=&v"(g1), "=&v"(g2), "=&v"(g3) : "v"(qo), "v"(qo2), "s"(s_ws), "s"(k4) : "memory");
        }
#pragma unroll
        for (int i = NV1; i < 8; ++i) dma(LB + L_B + (w * 8 + i) * 1024, vvo ^ (unsigned)((i & 1) * 128), s_v, kvb + i * 1024);
        v4u kf[2][4];
#pragma unroll
        for (int T = 0; T < 2; ++T)
#pragma unroll
            for (int st = 0; st < 4; ++st)
                kf[T][st] = lds_ld<v4u>(LB + L_A + (unsigned)(w * 32 + T * 16 + c) * 256u + (unsigned)(((st * 4 + gq) ^ c) * 16));
        if (w < 4) {
            asm volatile("s_waitcnt vmcnt(%4)" : "+v"(g0), "+v"(g1), "+v"(g2), "+v"(g3) : "n"(8 - NV1) : "memory");
            int spins = 0;
            while (!__all(tag_ok(g0, tag) & tag_ok(g1, tag) & tag_ok(g2, tag) & tag_ok(g3, tag)) && ++spins < SPIN_MAX) {
                const unsigned k4 = 4096u;
                asm volatile("buffer_load_dwordx4 %0, %4, %6, 0 offen sc1\n\t"
                             "buffer_load_dwordx4 %1, %4, %6, %7 offen sc1\n\t"
                             "buffer_load_dwordx4 %2, %5, %6, 0 offen sc1\n\t"
                             "buffer_load_dwordx4 %3, %5, %6, %7 offen sc1\n\t"
                             "s_waitcnt vmcnt(0)"
                             : "=&v"(g0), "=&v"(g1), "=&v"(g2), "=&v"(g3) : "v"(qo), "v"(qo2), "s"(s_ws), "s"(k4) : "memory");
            }
            const float qa = (__uint_as_float(g0.x) + __uint_as_float(g1.x)) + (__uint_as_float(g2.x) + __uint_as_float(g3.x));
            const float qb = (__uint_as_float(g0.z) + __uint_as_float(g1.z)) + (__uint_as_float(g2.z) + __uint_as_float(g3.z));
            lds_st<unsigned>(LB + L_QI + w * 320 + l * 4, cvtpk(qa, qb));
        }
        BAR();
        ST(4);
        v4f sc[2] = {{0.f, 0.f, 0.f, 0.f}, {0.f, 0.f, 0.f, 0.f}};
#pragma unroll
        for (int st = 0; st < 4; ++st) {
            const v4u qf = lds_ld<v4u>(LB + L_QI + (c & 3) * 320 + st * 64 + gq * 16);
            sc[0] = mfma(kf[0][st], qf, sc[0]);
            sc[1] = mfma(kf[1][st], qf, sc[1]);
        }
        float sv[8];
#pragma unroll
        for (int i = 0; i < 4; ++i) {
            const float a0 = sc[i >> 1][(i & 1) * 2], a1 = sc[i >> 1][(i & 1) * 2 + 1];
            const unsigned r1 = cvtpk(a0, a1);
            const unsigned r2 = cvtpk(bf_lo(r1) * 0.08838834764831845f, bf_hi(r1) * 0.08838834764831845f);
            sv[2 * i] = bf_lo(r2); sv[2 * i + 1] = bf_hi(r2);
        }
        float mx = fmaxf(fmaxf(fmaxf(sv[0], sv[1]), fmaxf(sv[2], sv[3])), fmaxf(fmaxf(sv[4], sv[5]), fmaxf(sv[6], sv[7])));
        mx = x32_max(x16_max(mx));
        if (l < 4) lds_st<float>(LB + L_MX + l * 32 + w * 4, mx);
        BAR();
        {
            const v4f m0 = lds_ld<v4f>(LB + L_MX + (c & 3) * 32), m1 = lds_ld<v4f>(LB + L_MX + (c & 3) * 32 + 16);
            mx = fmaxf(fmaxf(fmaxf(m0.x, m0.y), fmaxf(m0.z, m0.w)), fmaxf(fmaxf(m1.x, m1.y), fmaxf(m1.z, m1.w)));
        }
        float e[8], ls = 0.f;
#pragma unroll
        for (int i = 0; i < 8; ++i) { e[i] = __expf(sv[i] - mx); ls += e[i]; }
        ls = x32_sum(x16_sum(ls));
        if (l < 4) lds_st<float>(LB + L_LS + l * 32 + w * 4, ls);
        const v4u pf = {cvtpk(e[0], e[1]), cvtpk(e[2], e[3]), cvtpk(e[4], e[5]), cvtpk(e[6], e[7])};
        v4u pm[4];
#pragma unroll
        for (int xx = 0; xx < 4; ++xx) { const unsigned m = ((c >> 2) == xx) ? 0xffffffffu : 0u; pm[xx] = pf & m; }
        BAR();
        if (w == 7) {
            const v4f a0 = lds_ld<v4f>(LB + L_LS + (c & 3) * 32), a1 = lds_ld<v4f>(LB + L_LS + (c & 3) * 32 + 16);
            const float lt = ((a0.x + a0.y) + (a0.z + a0.w)) + ((a1.x + a1.y) + (a1.z + a1.w));
            if (l < 4) put16(s_ws, WS_ML + (unsigned)((b * 8 + xg) * 8 + s) * 128u + (unsigned)l * 16u, __float_as_uint(mx), __float_as_uint(lt), tag);
            WAITV(1);
        } else {
            WAITV(0);
        }
        ST(5);
        {
            const unsigned row = (unsigned)(w * 32 + gq * 4) + q4;
            const unsigned vb = LB + L_B + row * 256u + p4 * 8u;
            const unsigned sw = row & 7u;
            v4f oa = {0.f, 0.f, 0.f, 0.f}, ob = {0.f, 0.f, 0.f, 0.f};
#pragma unroll
            for (int xx = 0; xx < 4; ++xx) {
                const unsigned a0 = vb + (((unsigned)xx ^ sw) * 32u), a1 = vb + (((unsigned)(xx + 4) ^ sw) * 32u);
                oa = mfma(frag_tr(a0, a0 + 4096), pm[xx], oa);
                ob = mfma(frag_tr(a1, a1 + 4096), pm[xx], ob);
            }
            const unsigned ra = LB + L_C + (unsigned)w * 2048u + (unsigned)((c & 3) * 128 + (c >> 2) * 16 + gq * 4) * 4u;
            lds_st<v4f>(ra, oa);
            lds_st<v4f>(ra + 256, ob);
        }
        BAR();
        if (w < 4) {
            const unsigned un = (unsigned)(w * 32 + (l & 31));
            const unsigned ra = LB + L_C + (unsigned)(l >> 5) * 8192u + un * 16u;
            const v4f a0 = lds_ld<v4f>(ra), a1 = lds_ld<v4f>(ra + 2048), a2 = lds_ld<v4f>(ra + 4096), a3 = lds_ld<v4f>(ra + 6144);
            const v4f t = (a0 + a1) + (a2 + a3);
            const float o0 = x32_sum(t.x), o1 = x32_sum(t.y), o2 = x32_sum(t.z), o3 = x32_sum(t.w);
            const unsigned off = WS_O + (unsigned)(b * 8 + xg) * 16384u + (un >> 4) * 2048u + (unsigned)s * 256u + (un & 15u) * 16u;
            if (l < 32) put16(s_ws, off, cvtpk(o0, o1), cvtpk(o2, o3), tag);
        }
        ST(6);
        if (s == 7) goto done;
        __builtin_amdgcn_s_barrier();
        ST(12);
        {
            const int fb = xg * 7 + s;
            const v4i s_w1 = mksrd(w1, 1835008u), s_w2 = mksrd(w2, 1835008u);
            {
                const unsigned vo = (unsigned)(l >> 2) * 3584u + (unsigned)(((l & 3) ^ ((l >> 5) << 1)) * 16);
                const unsigned so = (unsigned)(w * 64) * 3584u + (unsigned)fb * 64u;
#pragma unroll
                for (int i = 0; i < 4; ++i) dma<NT1>(LB + L_A + (w * 4 + i) * 1024, vo, s_w1, so + i * 16 * 3584);
                const unsigned vo2 = (unsigned)(l >> 3) * 1024u + p8;
                const unsigned so2 = (unsigned)(fb * 32) * 1024u + (unsigned)w * 128u;
#pragma unroll
                for (int i = 0; i < 4; ++i) dma<NT2>(LB + L_A + 32768 + (w * 4 + i) * 1024, vo2 ^ (unsigned)((i & 1) * 64), s_w2, so2 + i * 8192);
            }
            const unsigned hs = LB + L_B + (unsigned)w * 4096u;
            {
                unsigned xr;
                const unsigned xo = (unsigned)(b * 512 + w * 64 + (l & 31) * 2) * 2u;
                asm volatile("buffer_load_dword %0, %1, %2, 0 offen\n\ts_waitcnt vmcnt(0)" : "=&v"(xr) : "v"(xo), "s"(s_x) : "memory");
                const v4u z4 = {0u, 0u, 0u, 0u};
                lds_st<v4u>(hs + l * 16, z4); lds_st<v4u>(hs + 1024 + l * 16, z4); lds_st<v4u>(hs + 2048 + l * 16, z4);
                lds_st<unsigned>(hs + (l < 32 ? 8 : 12) * 160 + (l & 31) * 4, l < 32 ? xr : 0x3f803f80u);
            }
            v4u f1[2][2], f2[4];
            {
                const unsigned a = LB + L_A + (unsigned)(w * 64 + gq * 8 + q4) * 64u + p4 * 8u;
                const unsigned sx = (unsigned)(gq & 1) * 32u;
#pragma unroll
                for (int ks = 0; ks < 2; ++ks) {
                    f1[ks][0] = frag_tr(a + sx + ks * 2048, a + sx + ks * 2048 + 256);
                    f1[ks][1] = frag_tr(a + (sx ^ 32u) + ks * 2048, a + (sx ^ 32u) + ks * 2048 + 256);
                }
                const unsigned a2 = LB + L_A + 32768 + (unsigned)w * 4096u + tr128;
#pragma unroll
                for (int nt = 0; nt < 4; ++nt) { const unsigned q = a2 + (((unsigned)nt ^ trsw) * 32u); f2[nt] = frag_tr(q, q + 512); }
            }
            const unsigned ha = hs + (unsigned)c * 160u + (unsigned)gq * 16u;
            float xw, cs;
            {
                v4f d0 = {0.f, 0.f, 0.f, 0.f}, d1 = {0.f, 0.f, 0.f, 0.f};
#pragma unroll
                for (int ks = 0; ks < 2; ++ks) { const v4u xf = lds_ld<v4u>(ha + ks * 64); d0 = mfma(xf, f1[ks][0], d0); d1 = mfma(xf, f1[ks][1], d1); }
                if (gq >= 2) { const unsigned a = LB + L_ZE + (unsigned)(w * 2 + (gq & 1)) * 128u + (unsigned)c * 4u; lds_st<float>(a, d0.x); lds_st<float>(a + 64, d1.x); }
                BAR();
                const unsigned a = LB + L_ZE + (unsigned)(c >> 3) * 1024u + (unsigned)(gq * 8 + (c & 7)) * 4u;
                xw = (lds_ld<float>(a) + lds_ld<float>(a + 256)) + (lds_ld<float>(a + 512) + lds_ld<float>(a + 768));
                cs = (lds_ld<float>(a + 128) + lds_ld<float>(a + 384)) + (lds_ld<float>(a + 640) + lds_ld<float>(a + 896));
                xw += dppf<XOR8>(xw); cs += dppf<XOR8>(cs);
            }
            ST(7);
            {
                v4u h0, h1;
                const unsigned ho = WS_H + (unsigned)(b * 8 + w) * 2048u + (unsigned)l * 16u;
                int spins = 0;
                do {
                    asm volatile("buffer_load_dwordx4 %0, %2, %3, 0 offen sc1\n\t"
                                 "buffer_load_dwordx4 %1, %2, %3, 0 offen offset:1024 sc1\n\t"
                                 "s_waitcnt vmcnt(0)"
                                 : "=&v"(h0), "=&v"(h1) : "v"(ho), "s"(s_ws) : "memory");
                } while (!__all(tag_ok(h0, tag) & tag_ok(h1, tag)) && ++spins < SPIN_MAX);
                const v2u a0 = {h0.x, h0.z}, a1 = {h1.x, h1.z};
                lds_st<v2u>(hs + (unsigned)gq * 160u + (unsigned)c * 8u, a0);
                lds_st<v2u>(hs + (unsigned)(gq + 4) * 160u + (unsigned)c * 8u, a1);
            }
            ST(8);
            {
                v4f d0 = {0.f, 0.f, 0.f, 0.f}, d1 = {0.f, 0.f, 0.f, 0.f}, gm = {0.f, 0.f, 0.f, 0.f};
#pragma unroll
                for (int ks = 0; ks < 2; ++ks) {
                    const v4u xf = lds_ld<v4u>(ha + ks * 64);
                    gm = mfma(xf, xf, gm); d0 = mfma(xf, f1[ks][0], d0); d1 = mfma(xf, f1[ks][1], d1);
                }
                float gs = (gm.x + gm.y) + (gm.z + gm.w);
                gs = sum16(c == 12 ? 0.f : gs);
                const float z0 = x16_sum((d0.x + d0.y) + (d0.z + d0.w)), z1 = x16_sum((d1.x + d1.y) + (d1.z + d1.w));
                if (c == 0) lds_st<float>(LB + L_GS + w * 16 + gq * 4, gs);
                if (l < 16) { lds_st<float>(LB + L_ZR + w * 128 + l * 4, z0); lds_st<float>(LB + L_ZR + w * 128 + 64 + l * 4, z1); }
            }
            BAR();
            {
                const v4f g4 = lds_ld<v4f>(LB + L_GS + (l & 7) * 16);
                const unsigned a = LB + L_ZR + (unsigned)(c >> 3) * 512u + (unsigned)(gq * 8 + (c & 7)) * 4u;
                float z = (lds_ld<float>(a) + lds_ld<float>(a + 128)) + (lds_ld<float>(a + 256) + lds_ld<float>(a + 384));
                const float s2 = sum8((g4.x + g4.y) + g4.z) * (1.0f / 512.0f), mean = sum8(g4.w) * (1.0f / 512.0f);
                z += dppf<XOR8>(z);
                const float var = fmaxf(s2 - mean * mean, 0.f);
                const float mr = rb(mean), den = rb(__fsqrt_rn(rb(rb(var) + 1e-5f)));
                z = rb(((z + xw) - mr * cs) * __builtin_amdgcn_rcpf(den));
                const float hv = z * __builtin_amdgcn_rcpf(1.0f + __expf(-z));
                const unsigned pk = cvtpk(hv, dppf<0x101>(hv));
                const v4u af = {pk, dppu<0x102>(pk), dppu<0x104>(pk), dppu<0x106>(pk)};
                const v4f zz = {0.f, 0.f, 0.f, 0.f};
                const v4f y0 = mfma(af, f2[0], zz), y1 = mfma(af, f2[1], zz), y2 = mfma(af, f2[2], zz), y3 = mfma(af, f2[3], zz);
                const bool hi = gq >= 2;
                const float ya = hi ? y2.x : y0.x, yb = hi ? y3.x : y1.x;
                const unsigned rblk = (unsigned)(b * 32 + w * 4 + (gq >> 1) * 2 + (c >> 3));
                const unsigned off = WS_Y + (rblk * 56u + (unsigned)fb) * 128u + (unsigned)(c & 7) * 16u;
                if ((gq & 1) == 0) put16(s_ws, off, __float_as_uint(ya), __float_as_uint(yb), tag);
            }
            ST(9);
        }
    } else {
        if (WOD) __builtin_amdgcn_s_sleep(WOD);
        {
            const v4i s_wo = mksrd(wo, 4u << 20);
            const unsigned vo = (unsigned)(l >> 3) * 1024u + p8;
            const unsigned so = (unsigned)(xg * 512 + w * 64) * 1024u + (unsigned)s * 128u;
#pragma unroll
            for (int i = 0; i < 8; ++i) dma<NTO>(LB + L_A + (w * 8 + i) * 1024, vo ^ (unsigned)((i & 1) * 64), s_wo, so + i * 8192);
        }
        WAITV(0);
        BAR();
        ST(3);
        v4u wf[16];
        float wgt[4];
        if (w < 4) {
            const unsigned a = LB + L_A + tr128 + (((unsigned)w ^ trsw) * 32u);
#pragma unroll
            for (int ks = 0; ks < 16; ++ks) wf[ks] = frag_tr(a + ks * 4096, a + ks * 4096 + 512);
            v4u ml;
            const unsigned mo = WS_ML + (unsigned)((b * 8 + xg) * 8 + (c & 7)) * 128u + (unsigned)gq * 16u;
            int spins = 0;
            do {
                asm volatile("buffer_load_dwordx4 %0, %1, %2, 0 offen sc1\n\ts_waitcnt vmcnt(0)" : "=&v"(ml) : "v"(mo), "s"(s_ws) : "memory");
            } while (!__all(tag_ok(ml, tag)) && ++spins < SPIN_MAX);
            const float m = __uint_as_float(ml.x), M = max8(m);
            const float f = __expf(m - M);
            const float L = sum8(f * __uint_as_float(ml.z));
            const float wv = f / L;
#pragma unroll
            for (int h = 0; h < 4; ++h) wgt[h] = __builtin_bit_cast(float, __builtin_amdgcn_ds_bpermute((h * 16 + c) * 4, __builtin_bit_cast(int, wv)));
        }
        BAR();
        ST(4);
        {
            v4u o0, o1;
            const unsigned oo = WS_O + (unsigned)(b * 8 + xg) * 16384u + (unsigned)w * 2048u + (unsigned)l * 16u;
            int spins = 0;
            do {
                asm volatile("buffer_load_dwordx4 %0, %2, %3, 0 offen sc1\n\t"
                             "buffer_load_dwordx4 %1, %2, %3, 0 offen offset:1024 sc1\n\t"
                             "s_waitcnt vmcnt(0)"
                             : "=&v"(o0), "=&v"(o1) : "v"(oo), "s"(s_ws) : "memory");
            } while (!__all(tag_ok(o0, tag) & tag_ok(o1, tag)) && ++spins < SPIN_MAX);
            const v2u a0 = {o0.x, o0.z}, a1 = {o1.x, o1.z};
            const unsigned sa = LB + L_B + (unsigned)gq * 1056u + (unsigned)(w * 64 + c * 4) * 2u;
            lds_st<v2u>(sa, a0);
            lds_st<v2u>(sa + 4 * 1056, a1);
        }
        BAR();
        ST(5);
        if (w < 4) {
            const unsigned oa = LB + L_B + (unsigned)(c & 7) * 1056u + (unsigned)gq * 16u;
            v4u of[16];
#pragma unroll
            for (int k = 0; k < 16; ++k) of[(k & 3) * 4 + (k >> 2)] = lds_ld<v4u>(oa + ((k & 3) * 4 + (k >> 2)) * 64);
            __builtin_amdgcn_sched_barrier(0);
            v4f acc[4] = {{0.f, 0.f, 0.f, 0.f}, {0.f, 0.f, 0.f, 0.f}, {0.f, 0.f, 0.f, 0.f}, {0.f, 0.f, 0.f, 0.f}};
#pragma unroll
            for (int k = 0; k < 4; ++k)
#pragma unroll
                for (int h = 0; h < 4; ++h) acc[h] = mfma(wf[h * 4 + k], of[h * 4 + k], acc[h]);
            __builtin_amdgcn_sched_barrier(0);
            const v4f t = (acc[0] * wgt[0] + acc[1] * wgt[1]) + (acc[2] * wgt[2] + acc[3] * wgt[3]);
            const float r0 = sum8(t.x), r1 = sum8(t.y), r2 = sum8(t.z), r3 = sum8(t.w);
            const unsigned off = WS_H + (unsigned)((b * 8 + s) * 8 + xg) * 256u + (unsigned)(w * 4 + gq) * 16u;
            if (c == 0) put16(s_ws, off, cvtpk(r0, r1), cvtpk(r2, r3), tag);
            ST(6);
        } else if (w == 4 && s < 4) {
            const int e = s >> 1, nh = s & 1, n7 = l & 7;
            const int col = xg * 64 + e * 32 + nh * 8 + n7;
            unsigned xr0, xr1; v4u h0, h1;
            const unsigned xo = (unsigned)(b * 512 + col) * 2u;
            const unsigned ho = WS_H + (unsigned)((b * 8 + xg) * 8 + (l >> 3)) * 256u + (unsigned)(e * 8 + nh * 2 + (n7 >> 2)) * 16u;
            asm volatile("buffer_load_ushort %0, %2, %3, 0 offen\n\tbuffer_load_ushort %1, %2, %3, 0 offen offset:32\n\ts_waitcnt vmcnt(0)"
                         : "=&v"(xr0), "=&v"(xr1) : "v"(xo), "s"(s_x) : "memory");
            int spins = 0;
            do {
                asm volatile("buffer_load_dwordx4 %0, %2, %3, 0 offen sc1\n\t"
                             "buffer_load_dwordx4 %1, %2, %3, 0 offen offset:64 sc1\n\t"
                             "s_waitcnt vmcnt(0)"
                             : "=&v"(h0), "=&v"(h1) : "v"(ho), "s"(s_ws) : "memory");
            } while (!__all(tag_ok(h0, tag) & tag_ok(h1, tag)) && ++spins < SPIN_MAX);
            const unsigned d0 = (n7 & 2) ? h0.z : h0.x, d1 = (n7 & 2) ? h1.z : h1.x;
            float a0 = (n7 & 1) ? bf_hi(d0) : bf_lo(d0), a1 = (n7 & 1) ? bf_hi(d1) : bf_lo(d1);
            a0 += dppf<XOR8>(a0); a1 += dppf<XOR8>(a1);
            a0 = x32_sum(x16_sum(a0)); a1 = x32_sum(x16_sum(a1));
            const float hh0 = rb(bf_lo(xr0) + rb(a0)), hh1 = rb(bf_lo(xr1) + rb(a1));
            ST(7);
            v4u y[7];
            const unsigned yo = WS_Y + (unsigned)(b * 32 + xg * 4 + s) * 7168u + (unsigned)l * 16u, yo2 = yo + 4096u;
            spins = 0;
            bool ok;
            do {
                asm volatile("buffer_load_dwordx4 %0, %7, %9, 0 offen sc1\n\t"
                             "buffer_load_dwordx4 %1, %7, %9, 0 offen offset:1024 sc1\n\t"
                             "buffer_load_dwordx4 %2, %7, %9, 0 offen offset:2048 sc1\n\t"
                             "buffer_load_dwordx4 %3, %7, %9, 0 offen offset:3072 sc1\n\t"
                             "buffer_load_dwordx4 %4, %8, %9, 0 offen sc1\n\t"
                             "buffer_load_dwordx4 %5, %8, %9, 0 offen offset:1024 sc1\n\t"
                             "buffer_load_dwordx4 %6, %8, %9, 0 offen offset:2048 sc1\n\t"
                             "s_waitcnt vmcnt(0)"
                             : "=&v"(y[0]), "=&v"(y[1]), "=&v"(y[2]), "=&v"(y[3]), "=&v"(y[4]), "=&v"(y[5]), "=&v"(y[6])
                             : "v"(yo), "v"(yo2), "s"(s_ws) : "memory");
                ok = true;
#pragma unroll
                for (int k = 0; k < 7; ++k) ok &= tag_ok(y[k], tag);
            } while (!__all(ok) && ++spins < SPIN_MAX);
            ST(8);
            float ya = 0.f, yb = 0.f;
#pragma unroll
            for (int k = 0; k < 7; ++k) { ya += __uint_as_float(y[k].x); yb += __uint_as_float(y[k].z); }
            ya += dppf<XOR8>(ya); yb += dppf<XOR8>(yb);
            ya = x32_sum(x16_sum(ya)); yb = x32_sum(x16_sum(yb));
            const unsigned r = cvtpk(hh0 + rb(ya), hh1 + rb(yb));
            if (l < 8) { out[b * 512 + col] = (u16)r; out[b * 512 + col + 16] = (u16)(r >> 16); }
            if (blockIdx.x == 0 && l == 0) {
                const unsigned zo = 0;
                asm volatile("buffer_store_dword %0, %1, %2, 0 offen sc1" ::"v"(tag), "v"(zo), "s"(s_ws) : "memory");
            }
            ST(9);
        }
    }
done:;
}

extern "C" void kernel_launch(void* const* d_in, const int* in_sizes, int n_in, void* d_out, int out_size, void* d_ws, size_t ws_size,
                              hipStream_t stream) {
    fused_block<<<256, 512, 0, stream>>>((const u16*)d_in[0], (const u16*)d_in[1], (const u16*)d_in[2], (const u16*)d_in[3], (const u16*)d_in[4],
                                         (const u16*)d_in[6], (const u16*)d_in[7], (u16*)d_out, (unsigned char*)d_ws);
}
```

```cpp
#include <hip/hip_runtime.h>
#include <hip/hip_bf16.h>

typedef unsigned short u16;
typedef unsigned long long u64;
typedef unsigned v4u __attribute__((ext_vector_type(4)));
typedef unsigned v2u __attribute__((ext_vector_type(2)));
typedef int v4i __attribute__((ext_vector_type(4)));
typedef float v4f __attribute__((ext_vector_type(4)));
typedef float v2f __attribute__((ext_vector_type(2)));
typedef short v4s __attribute__((ext_vector_type(4)));
typedef __bf16 v8bf __attribute__((ext_vector_type(8)));
typedef __bf16 v2bf __attribute__((ext_vector_type(2)));
#define LAS __attribute__((address_space(3)))
#define DEV __device__ __forceinline__

#ifndef NV1
#define NV1 4
#endif
#define WOD 40
#define MAP 1
#define MLPW 1
#define NTA 1
#define NT1 1
#define NT2 1
#define NTO 1
#define HD 0

constexpr unsigned WS_Q = 4096;
constexpr unsigned WS_ML = WS_Q + 262144;
constexpr unsigned WS_O = WS_ML + 16384;
constexpr unsigned WS_H = WS_O + 262144;
constexpr unsigned WS_Y = WS_H + 32768;
constexpr unsigned WS_END = WS_Y + 458752;

constexpr unsigned L_A = 0;
constexpr unsigned L_B = 65536;
constexpr unsigned L_C = 131072;
constexpr unsigned L_LN = 147456;
constexpr unsigned L_QI = L_LN + 640;
constexpr unsigned L_Z = L_QI + 1280;
constexpr unsigned L_ONE = L_Z + 256;
constexpr unsigned L_MX = L_ONE + 128;
constexpr unsigned L_W = L_MX + 64;
constexpr unsigned L_P = L_W + 192;
constexpr unsigned L_OS = L_P + 2048;
constexpr unsigned L_HR = L_OS + 1024;
constexpr unsigned L_ZR = L_HR + 2048;
constexpr unsigned L_GS = L_ZR + 2560;
constexpr unsigned L_ZE = L_GS + 128;
constexpr unsigned L_DM = L_ZE + 2048;
constexpr unsigned L_END = L_DM + 1536;

DEV float bf_lo(unsigned v) { return __uint_as_float(v << 16); }
DEV float bf_hi(unsigned v) { return __uint_as_float(v & 0xffff0000u); }
DEV unsigned cvtpk(float a, float b) { v2f t = {a, b}; return __builtin_bit_cast(unsigned, __builtin_convertvector(t, v2bf)); }
DEV float rb(float a) { return bf_lo(cvtpk(a, a)); }
DEV float vmax(float a, float b) { return __builtin_elementwise_maximum(a, b); }
DEV float vmax3(float a, float b, float c) { return vmax(a, vmax(b, c)); }
template <int CTRL> DEV float dppf(float x) { return __builtin_bit_cast(float, __builtin_amdgcn_mov_dpp(__builtin_bit_cast(int, x), CTRL, 0xf, 0xf, true)); }
template <int CTRL> DEV unsigned dppu(unsigned x) { return (unsigned)__builtin_amdgcn_mov_dpp((int)x, CTRL, 0xf, 0xf, true); }
constexpr int XOR1 = 0xB1, XOR2 = 0x4E, XOR7 = 0x141, XOR8 = 0x128;
DEV float mix16(float a, float b) { auto s = __builtin_amdgcn_permlane16_swap(__float_as_uint(a), __float_as_uint(b), false, false); return __uint_as_float(s[0]) + __uint_as_float(s[1]); }
DEV float mix32(float a, float b) { auto s = __builtin_amdgcn_permlane32_swap(__float_as_uint(a), __float_as_uint(b), false, false); return __uint_as_float(s[0]) + __uint_as_float(s[1]); }
DEV float x16_sum(float x) { return mix16(x, x); }
DEV float x32_sum(float x) { return mix32(x, x); }
DEV float x16_max(float x) { auto s = __builtin_amdgcn_permlane16_swap(__float_as_uint(x), __float_as_uint(x), false, false); return vmax(__uint_as_float(s[0]), __uint_as_float(s[1])); }
DEV float x32_max(float x) { auto s = __builtin_amdgcn_permlane32_swap(__float_as_uint(x), __float_as_uint(x), false, false); return vmax(__uint_as_float(s[0]), __uint_as_float(s[1])); }
DEV float sum8(float x) { x += dppf<XOR1>(x); x += dppf<XOR2>(x); x += dppf<XOR7>(x); return x; }
DEV float sum16(float x) { x = sum8(x); x += dppf<XOR8>(x); return x; }
DEV float dot2(unsigned a, unsigned b, float c) { return __builtin_amdgcn_fdot2_f32_bf16(__builtin_bit_cast(v2bf, a), __builtin_bit_cast(v2bf, b), c, false); }
DEV v4f mfma(v4u a, v4u b, v4f c) { return __builtin_amdgcn_mfma_f32_16x16x32_bf16(__builtin_bit_cast(v8bf, a), __builtin_bit_cast(v8bf, b), c, 0, 0, 0); }
DEV v4i mksrd(const void* p, unsigned n) { u64 a = (u64)p; v4i r; r.x = (int)(unsigned)a; r.y = (int)(unsigned)(a >> 32); r.z = (int)n; r.w = 0x00020000; return r; }

template <typename T> DEV T lds_ld(unsigned a) { return *(LAS T*)(size_t)a; }
template <typename T> DEV void lds_st(unsigned a, T v) { *(LAS T*)(size_t)a = v; }
DEV v2u lds_tr(unsigned a) { return __builtin_bit_cast(v2u, __builtin_amdgcn_ds_read_tr16_b64_v4i16((LAS v4s*)(size_t)a)); }
DEV v4u frag_tr(unsigned a0, unsigned a1) { v2u p = lds_tr(a0), q = lds_tr(a1); v4u r = {p.x, p.y, q.x, q.y}; return r; }
#define PIN(x) asm volatile("" : "+v"(x))

template <int NT> DEV void dma(unsigned ldsdst, unsigned voff, v4i srd, unsigned soff) {
    if (NT) asm volatile("s_mov_b32 m0, %0\n\ts_nop 0\n\tbuffer_load_dwordx4 %1, %2, %3 offen nt lds" ::"s"(ldsdst), "v"(voff), "s"(srd), "s"(soff) : "memory");
    else asm volatile("s_mov_b32 m0, %0\n\ts_nop 0\n\tbuffer_load_dwordx4 %1, %2, %3 offen lds" ::"s"(ldsdst), "v"(voff), "s"(srd), "s"(soff) : "memory");
}
#define WAITV(n) asm volatile("s_waitcnt vmcnt(%0)" ::"n"(n) : "memory")
#define BAR() asm volatile("s_waitcnt lgkmcnt(0)\n\ts_barrier" ::: "memory")
constexpr unsigned OOB = 0x7ff00000u;
DEV void put16(v4i srd, unsigned voff, unsigned a, unsigned b, unsigned tagv) {
    v4u v = {a, tagv, b, tagv};
    asm volatile("buffer_store_dwordx4 %0, %1, %2, 0 offen sc1\n\ts_nop 1" ::"v"(v), "v"(voff), "s"(srd) : "memory");
}
DEV void put8(v4i srd, unsigned voff, unsigned a, unsigned tagv) {
    v2u v = {a, tagv};
    asm volatile("buffer_store_dwordx2 %0, %1, %2, 0 offen sc1\n\ts_nop 1" ::"v"(v), "v"(voff), "s"(srd) : "memory");
}

#define SPINS "0x3000"
DEV void poll1(v4u& r0, unsigned off, v4i srd, unsigned tag) {
    unsigned t, cnt;
    asm volatile("s_movk_i32 %[cnt], " SPINS "\n"
                 "1:\n\t"
                 "buffer_load_dwordx4 v[160:163], %[off], %[srd], 0 offen sc1\n\t"
                 "s_sub_u32 %[cnt], %[cnt], 1\n\t"
                 "s_waitcnt vmcnt(0)\n\t"
                 "s_cbranch_scc1 2f\n\t"
                 "v_bitop3_b32 %[t], v161, v163, %[tag] bitop3:0x7e\n\t"
                 "v_cmp_ne_u32 vcc, 0, %[t]\n\t"
                 "s_cbranch_vccnz 1b\n"
                 "2:"
                 : [t] "=&v"(t), [cnt] "=&s"(cnt), "=&{v[160:163]}"(r0) : [off] "v"(off), [srd] "s"(srd), [tag] "s"(tag) : "memory", "vcc", "scc");
}
DEV void poll2(v4u& r0, v4u& r1, unsigned off, v4i srd, unsigned tag) {
    unsigned t, t2, cnt;
    asm volatile("s_movk_i32 %[cnt], " SPINS "\n"
                 "1:\n\t"
                 "buffer_load_dwordx4 v[160:163], %[off], %[srd], 0 offen sc1\n\t"
                 "buffer_load_dwordx4 v[164:167], %[off], %[srd], 0 offen offset:1024 sc1\n\t"
                 "s_sub_u32 %[cnt], %[cnt], 1\n\t"
                 "s_waitcnt vmcnt(0)\n\t"
                 "s_cbranch_scc1 2f\n\t"
                 "v_bitop3_b32 %[t], v161, v163, %[tag] bitop3:0x7e\n\t"
                 "v_bitop3_b32 %[t2], v165, v167, %[tag] bitop3:0x7e\n\t"
                 "v_or_b32 %[t], %[t], %[t2]\n\t"
                 "v_cmp_ne_u32 vcc, 0, %[t]\n\t"
                 "s_cbranch_vccnz 1b\n"
                 "2:"
                 : [t] "=&v"(t), [t2] "=&v"(t2), [cnt] "=&s"(cnt), "=&{v[160:163]}"(r0), "=&{v[164:167]}"(r1)
                 : [off] "v"(off), [srd] "s"(srd), [tag] "s"(tag) : "memory", "vcc", "scc");
}
DEV void poll2_lds(unsigned off, v4i srd, unsigned tag, unsigned la) {
    unsigned t, t2, cnt;
    asm volatile("s_movk_i32 %[cnt], " SPINS "\n"
                 "1:\n\t"
                 "buffer_load_dwordx4 v[160:163], %[off], %[srd], 0 offen sc1\n\t"
                 "buffer_load_dwordx4 v[164:167], %[off], %[srd], 0 offen offset:1024 sc1\n\t"
                 "s_sub_u32 %[cnt], %[cnt], 1\n\t"
                 "s_waitcnt vmcnt(0)\n\t"
                 "ds_write2_b32 %[la], v160, v162 offset1:1\n\t"
                 "ds_write2_b32 %[la], v164, v166 offset0:160 offset1:161\n\t"
                 "s_cbranch_scc1 2f\n\t"
                 "v_bitop3_b32 %[t], v161, v163, %[tag] bitop3:0x7e\n\t"
                 "v_bitop3_b32 %[t2], v165, v167, %[tag] bitop3:0x7e\n\t"
                 "v_or_b32 %[t], %[t], %[t2]\n\t"
                 "v_cmp_ne_u32 vcc, 0, %[t]\n\t"
                 "s_cbranch_vccz 2f\n\t"
                 "s_waitcnt lgkmcnt(0)\n\t"
                 "s_branch 1b\n"
                 "2:"
                 : [t] "=&v"(t), [t2] "=&v"(t2), [cnt] "=&s"(cnt)
                 : [off] "v"(off), [srd] "s"(srd), [tag] "s"(tag), [la] "v"(la)
                 : "memory", "vcc", "scc", "v160", "v161", "v162", "v163", "v164", "v165", "v166", "v167");
}

#define ST(i) do { } while (0)
#define STARG
#define STPASS

struct Ctx {
    int l, w, gq, c;
    unsigned q4, p4, p8, trsw, tr128;
    unsigned tag, tagv;
    v4i s_ws, s_x;
};

DEV void mlp_load1(const Ctx& k, unsigned MB, int fb, const u16* w1) {
    const v4i s_w1 = mksrd(w1, 1835008u);
    const unsigned vo = (unsigned)(k.l >> 2) * 3584u + (unsigned)(((k.l & 3) ^ ((k.l >> 5) << 1)) * 16);
    const unsigned so = (unsigned)(k.w * 64) * 3584u + (unsigned)fb * 64u;
#pragma unroll
    for (int i = 0; i < 4; ++i) dma<NT1>(MB + k.w * 8192 + i * 1024, vo, s_w1, so + i * 16 * 3584);
}
DEV void mlp_load2(const Ctx& k, unsigned MB, int fb, const u16* w2) {
    const v4i s_w2 = mksrd(w2, 1835008u);
    const unsigned vo2 = (unsigned)(k.l >> 3) * 1024u + k.p8;
    const unsigned so2 = (unsigned)(fb * 32) * 1024u + (unsigned)k.w * 128u;
#pragma unroll
    for (int i = 0; i < 4; ++i) dma<NT2>(MB + k.w * 8192 + 4096 + i * 1024, vo2 ^ (unsigned)((i & 1) * 64), s_w2, so2 + i * 8192);
}
struct Mlp { v4u f1[2][2], f2[4]; float xw, cs; unsigned ha; };
DEV void mlp_prep(const Ctx& k, unsigned MB, int bM, Mlp& m) {
    const int l = k.l, w = k.w, gq = k.gq, c = k.c;
    const unsigned hs = L_C + (unsigned)w * 1440u;
    {
        unsigned x0, x1;
        const unsigned xo = (unsigned)(bM * 512 + w * 64 + ((l >> 4) & 1) * 32 + c) * 2u;
        asm volatile("buffer_load_ushort %0, %2, %3, 0 offen\n\tbuffer_load_ushort %1, %2, %3, 0 offen offset:32\n\ts_waitcnt vmcnt(0)"
                     : "=&v"(x0), "=&v"(x1) : "v"(xo), "s"(k.s_x) : "memory");
        lds_st<unsigned>(hs + 8 * 160 + (l & 31) * 4, x0 | (x1 << 16));
    }
    {
        const unsigned a = MB + (unsigned)w * 8192u + ((k.q4 & 1) * 16 + (unsigned)gq * 4 + (k.q4 >> 1)) * 64u + k.p4 * 8u;
        const unsigned sx = (unsigned)(gq >> 1) * 32u;
#pragma unroll
        for (int ks = 0; ks < 2; ++ks) {
            m.f1[ks][0] = frag_tr(a + sx + ks * 2048, a + sx + ks * 2048 + 128);
            m.f1[ks][1] = frag_tr(a + (sx ^ 32u) + ks * 2048, a + (sx ^ 32u) + ks * 2048 + 128);
        }
        const unsigned a2 = MB + (unsigned)w * 8192u + 4096u + k.tr128;
#pragma unroll
        for (int nt = 0; nt < 4; ++nt) { const unsigned q = a2 + (((unsigned)nt ^ k.trsw) * 32u); m.f2[nt] = frag_tr(q, q + 512); }
    }
    m.ha = (c <= 8 ? hs + (unsigned)c * 160u : (c == 12 ? L_ONE : L_Z)) + (unsigned)gq * 16u;
    v4f d0 = {0.f, 0.f, 0.f, 0.f}, d1 = {0.f, 0.f, 0.f, 0.f};
#pragma unroll
    for (int ks = 0; ks < 2; ++ks) { const v4u xf = lds_ld<v4u>(m.ha + ks * 64); d0 = mfma(xf, m.f1[ks][0], d0); d1 = mfma(xf, m.f1[ks][1], d1); }
    if (gq >= 2) { const unsigned a = L_ZE + (unsigned)(w * 2 + (gq & 1)) * 128u + (unsigned)c * 4u; lds_st<float>(a, d0.x); lds_st<float>(a + 64, d1.x); }
    BAR();
    const unsigned a = L_ZE + (unsigned)(c >> 3) * 1024u + (unsigned)(gq * 8 + (c & 7)) * 4u;
    float xw = (lds_ld<float>(a) + lds_ld<float>(a + 256)) + (lds_ld<float>(a + 512) + lds_ld<float>(a + 768));
    float cs = (lds_ld<float>(a + 128) + lds_ld<float>(a + 384)) + (lds_ld<float>(a + 640) + lds_ld<float>(a + 896));
    m.xw = xw + dppf<XOR8>(xw); m.cs = cs + dppf<XOR8>(cs);
}
DEV void mlp_run(STARG const Ctx& k, int bM, int fb, Mlp& m) {
    const int l = k.l, w = k.w, gq = k.gq, c = k.c;
    unsigned ho = WS_H + (unsigned)(bM * 8 + w) * 2048u + (unsigned)l * 16u;
    unsigned sa = L_C + (unsigned)w * 1440u + (unsigned)gq * 160u + (unsigned)c * 8u;
    unsigned za = gq < 2 ? L_ZR + (unsigned)c * 80u + (unsigned)(w * 2 + gq) * 4u : L_DM + (unsigned)(l & 31) * 4u;
    unsigned ga = L_GS + (unsigned)(w * 16 + gq * 4);
    unsigned zr = L_ZR + (unsigned)(gq * 8 + (c & 7)) * 80u + (unsigned)(c >> 3) * 32u;
    unsigned gr = L_GS + (unsigned)(l & 7) * 16u;
    const unsigned rblk = (unsigned)(bM * 32 + w * 4 + (gq >> 1) * 2 + (c >> 3));
    unsigned yo = (gq & 1) ? OOB : WS_Y + (rblk * 56u + (unsigned)fb) * 128u + (unsigned)(c & 7) * 16u;
    float gmask = c == 12 ? 0.f : 1.f;
    PIN(ho); PIN(sa); PIN(za); PIN(ga); PIN(zr); PIN(gr); PIN(yo); PIN(gmask); PIN(m.xw); PIN(m.cs); PIN(m.ha);
    if (HD) __builtin_amdgcn_s_sleep(HD);
    ST(7);
    poll2_lds(ho, k.s_ws, k.tag, sa);
    ST(8);
    {
        const v4f zz = {0.f, 0.f, 0.f, 0.f};
        const v4u x0 = lds_ld<v4u>(m.ha), x1 = lds_ld<v4u>(m.ha + 64);
        v4f gm = mfma(x0, x0, zz);
        gm = mfma(x1, x1, gm);
        v4f d0 = mfma(x0, m.f1[0][0], zz), d1 = mfma(x0, m.f1[0][1], zz);
        d0 = mfma(x1, m.f1[1][0], d0); d1 = mfma(x1, m.f1[1][1], d1);
        const float gs = sum16(((gm.x + gm.y) + (gm.z + gm.w)) * gmask);
        lds_st<float>(ga, gs);
        lds_st<float>(za, (d0.x + d0.y) + (d0.z + d0.w));
        lds_st<float>(za + 1280, (d1.x + d1.y) + (d1.z + d1.w));
    }
    BAR();
    {
        const v4f g4 = lds_ld<v4f>(gr);
        const v4f za4 = lds_ld<v4f>(zr), zb4 = lds_ld<v4f>(zr + 16);
        const float s2 = sum8((g4.x + g4.y) + g4.z) * (1.0f / 512.0f), mean = sum8(g4.w) * (1.0f / 512.0f);
        const v4f zs = za4 + zb4;
        float z = (zs.x + zs.y) + (zs.z + zs.w);
        z += dppf<XOR8>(z);
        const float var = vmax(s2 - mean * mean, 0.f);
        const float mr = rb(mean), den = rb(__builtin_amdgcn_sqrtf(rb(rb(var) + 1e-5f)));
        z = rb(((z + m.xw) - mr * m.cs) * __builtin_amdgcn_rcpf(den));
        const float hv = z * __builtin_amdgcn_rcpf(1.0f + __builtin_amdgcn_exp2f(z * -1.4426950408889634f));
        const unsigned pk = cvtpk(hv, dppf<0x101>(hv));
        const v4u af = {pk, dppu<0x102>(pk), dppu<0x104>(pk), dppu<0x106>(pk)};
        const v4f zz = {0.f, 0.f, 0.f, 0.f};
        const v4f y0 = mfma(af, m.f2[0], zz), y2 = mfma(af, m.f2[2], zz), y1 = mfma(af, m.f2[1], zz), y3 = mfma(af, m.f2[3], zz);
        const bool hi = gq >= 2;
        put16(k.s_ws, yo, __float_as_uint(hi ? y2.x : y0.x), __float_as_uint(hi ? y3.x : y1.x), k.tagv);
    }
    ST(9);
}
DEV void fin_run(STARG const Ctx& k, int bF, int cF, int s, u16* out) {
    const int l = k.l, n7 = l & 7;
    const int col = cF * 64 + (s >> 1) * 32 + (s & 1) * 8 + n7;
    unsigned xr0, xr1;
    const unsigned xo = (unsigned)(bF * 512 + col) * 2u;
    asm volatile("buffer_load_ushort %0, %2, %3, 0 offen\n\tbuffer_load_ushort %1, %2, %3, 0 offen offset:32\n\ts_waitcnt vmcnt(0)"
                 : "=&v"(xr0), "=&v"(xr1) : "v"(xo), "s"(k.s_x) : "memory");
    v4u h0;
    poll1(h0, WS_H + (unsigned)((bF * 8 + cF) * 8 + (l >> 3)) * 256u + (unsigned)(s * 4 + (n7 >> 1)) * 16u, k.s_ws, k.tag);
    const unsigned d0 = (n7 & 1) ? h0.z : h0.x;
    float a = mix32(bf_lo(d0), bf_hi(d0));
    a += dppf<XOR8>(a);
    a = x16_sum(a);
    float hh = rb(bf_lo(l < 32 ? xr0 : xr1) + rb(a));
    unsigned yo = WS_Y + (unsigned)(bF * 32 + cF * 4 + s) * 7168u + (unsigned)l * 16u, yo2 = yo + 4096u;
    const v4i s_out = mksrd(out, 2048);
    unsigned oo = (l & 24) ? OOB : xo + (unsigned)(l >> 5) * 32u;
    PIN(hh); PIN(yo); PIN(yo2); PIN(oo);
    ST(7);
    v4u y0, y1, y2, y3, y4, y5, y6;
    unsigned t, t2, t3, cnt;
    asm volatile("s_movk_i32 %[cnt], " SPINS "\n"
                 "1:\n\t"
                 "buffer_load_dwordx4 v[160:163], %[o1], %[srd], 0 offen sc1\n\t"
                 "buffer_load_dwordx4 v[164:167], %[o1], %[srd], 0 offen offset:1024 sc1\n\t"
                 "buffer_load_dwordx4 v[168:171], %[o1], %[srd], 0 offen offset:2048 sc1\n\t"
                 "buffer_load_dwordx4 v[172:175], %[o1], %[srd], 0 offen offset:3072 sc1\n\t"
                 "buffer_load_dwordx4 v[176:179], %[o2], %[srd], 0 offen sc1\n\t"
                 "buffer_load_dwordx4 v[180:183], %[o2], %[srd], 0 offen offset:1024 sc1\n\t"
                 "buffer_load_dwordx4 v[184:187], %[o2], %[srd], 0 offen offset:2048 sc1\n\t"
                 "s_sub_u32 %[cnt], %[cnt], 1\n\t"
                 "s_waitcnt vmcnt(5)\n\t"
                 "v_bitop3_b32 %[t], v161, v163, %[tag] bitop3:0x7e\n\t"
                 "v_bitop3_b32 %[t2], v165, v167, %[tag] bitop3:0x7e\n\t"
                 "s_waitcnt vmcnt(3)\n\t"
                 "v_bitop3_b32 %[t3], v169, v171, %[tag] bitop3:0x7e\n\t"
                 "v_or3_b32 %[t], %[t], %[t2], %[t3]\n\t"
                 "v_bitop3_b32 %[t2], v173, v175, %[tag] bitop3:0x7e\n\t"
                 "s_waitcnt vmcnt(1)\n\t"
                 "v_bitop3_b32 %[t3], v177, v179, %[tag] bitop3:0x7e\n\t"
                 "v_or3_b32 %[t], %[t], %[t2], %[t3]\n\t"
                 "v_bitop3_b32 %[t2], v181, v183, %[tag] bitop3:0x7e\n\t"
                 "s_waitcnt vmcnt(0)\n\t"
                 "s_cbranch_scc1 2f\n\t"
                 "v_bitop3_b32 %[t3], v185, v187, %[tag] bitop3:0x7e\n\t"
                 "v_or3_b32 %[t], %[t], %[t2], %[t3]\n\t"
                 "v_cmp_ne_u32 vcc, 0, %[t]\n\t"
                 "s_cbranch_vccnz 1b\n"
                 "2:"
                 : [t] "=&v"(t), [t2] "=&v"(t2), [t3] "=&v"(t3), [cnt] "=&s"(cnt), "=&{v[160:163]}"(y0), "=&{v[164:167]}"(y1), "=&{v[168:171]}"(y2),
                   "=&{v[172:175]}"(y3), "=&{v[176:179]}"(y4), "=&{v[180:183]}"(y5), "=&{v[184:187]}"(y6)
                 : [o1] "v"(yo), [o2] "v"(yo2), [srd] "s"(k.s_ws), [tag] "s"(k.tag) : "memory", "vcc", "scc");
    ST(8);
#define F_(v) __uint_as_float(v)
    const float ya = ((F_(y0.x) + F_(y1.x)) + (F_(y2.x) + F_(y3.x))) + ((F_(y4.x) + F_(y5.x)) + F_(y6.x));
    const float yb = ((F_(y0.z) + F_(y1.z)) + (F_(y2.z) + F_(y3.z))) + ((F_(y4.z) + F_(y5.z)) + F_(y6.z));
    float y = mix32(ya, yb);
    y += dppf<XOR8>(y);
    y = x16_sum(y);
    const unsigned r = cvtpk(hh + rb(y), 0.f);
    asm volatile("buffer_store_short %0, %1, %2, 0 offen" ::"v"(r), "v"(oo), "s"(s_out) : "memory");
    if ((bF | cF | s) == 0) {
        const unsigned zo = l ? OOB : 0u;
        asm volatile("buffer_store_dword %0, %1, %2, 0 offen sc1" ::"v"(k.tagv), "v"(zo), "s"(k.s_ws) : "memory");
    }
    ST(9);
}

__global__ void __launch_bounds__(512, 2)
fused_block(const u16* __restrict__ x, const u16* __restrict__ kc, const u16* __restrict__ vc, const u16* __restrict__ wq,
            const u16* __restrict__ wo, const u16* __restrict__ w1, const u16* __restrict__ w2, u16* __restrict__ out,
            unsigned char* __restrict__ ws) {
    __shared__ __attribute__((aligned(1024))) unsigned char lds_raw[L_END];
    asm volatile("" ::"v"((LAS unsigned char*)lds_raw));
    const int tid = threadIdx.x, l = tid & 63;
    const int w = __builtin_amdgcn_readfirstlane(tid >> 6);
    const int xc = blockIdx.x & 7, u = blockIdx.x >> 3;
    const int gq = l >> 4, c = l & 15;
    const bool att = u >= 16;
    const int ub = (u >> 3) & 1, s = u & 7;
    const int kq = u & 3, cq = u >> 2;
    const int hi = xc >> 2, gi = (xc & 3) * 2 + ub;
    const int bA = 1 - hi, gA = gi, bP = hi, gP = gi, gQ = (2 * xc + 2 + hi) & 7, bM = 1 - hi, bF = hi, cF = gi, fb = gi * 7 + s;
    Ctx k;
    k.l = l; k.w = w; k.gq = gq; k.c = c;
    k.s_ws = mksrd(ws, WS_END); k.s_x = mksrd(x, 2048);
    unsigned ev;
    asm volatile("s_load_dword %0, %1, 0x0" : "=s"(ev) : "s"(ws), "s"(kc), "s"(vc), "s"(wq), "s"(wo), "s"(w1), "s"(w2), "s"(out) : "memory");

    unsigned xv; v4u xa;
    {
        const unsigned xo = (unsigned)((w >> 1) & 1) * 1024u + (unsigned)l * 16u, xvo = (unsigned)(((tid >> 7) & 1) * 512 + kq * 128 + (tid & 127)) * 2u;
        asm volatile("buffer_load_dwordx4 %0, %2, %3, 0 offen\n\t"
                     "buffer_load_ushort %1, %4, %3, 0 offen"
                     : "=&{v[160:163]}"(xa), "=&{v164}"(xv) : "v"(xo), "s"(k.s_x), "v"(xvo) : "memory");
    }
    k.p8 = (unsigned)(((l & 7) ^ (((l >> 4) & 1) << 1)) * 16);
    {
        const v4i s_wq = mksrd(wq, 4u << 20);
        const unsigned vo = (unsigned)(l >> 3) * 8192u + k.p8;
        const unsigned so = (unsigned)(kq * 128 + w * 16) * 8192u + (unsigned)(gQ * 512 + cq * 64) * 2u;
        dma<NTA>(L_C + w * 2048, vo, s_wq, so);
        dma<NTA>(L_C + w * 2048 + 1024, vo ^ 64u, s_wq, so + 8u * 8192u);
    }
    __builtin_amdgcn_s_barrier();
    const v4i s_k = mksrd(kc, 32u << 20), s_v = mksrd(vc, 32u << 20);
    const unsigned kvb = (unsigned)((bA * 8 + gA) * 8192 + 6144 + s * 256 + w * 32) * 256u;
    const unsigned kvo = (unsigned)gq * 256u + (unsigned)((c ^ gq) * 16);
    if (att) {
#pragma unroll
        for (int i = 0; i < 6; ++i) dma<NTA>(L_A + (w * 8 + i) * 1024, kvo ^ (unsigned)((i & 3) * 64), s_k, kvb + i * 1024);
    }
    k.q4 = (unsigned)((l >> 2) & 3); k.p4 = (unsigned)(l & 3);
    k.trsw = (k.q4 >> 1) | ((unsigned)(gq & 1) << 1);
    k.tr128 = (unsigned)(gq * 8 + k.q4) * 128u + k.p4 * 8u;
    unsigned qa = L_C + k.tr128 + (((unsigned)w ^ k.trsw) * 32u);
    unsigned la = L_LN + (unsigned)((c >> 2) & 1) * 320u + (unsigned)gq * 16u;
    unsigned qoff = l < 32 ? WS_Q + (unsigned)((((gQ * 2 + gq) * 4 + kq) * 512) + cq * 64 + w * 16 + c) * 8u : OOB;
    PIN(qa); PIN(la); PIN(qoff);
    asm volatile("s_cmp_lg_u32 %2, 0\n\ts_cbranch_scc1 1f\n\ts_waitcnt vmcnt(2)\n1:\n\ts_waitcnt vmcnt(8)"
                 : "+{v[160:163]}"(xa), "+{v164}"(xv) : "s"(u >> 4) : "memory", "scc");
    if (w < 4) {
        const float s1 = dot2(xa.w, 0x3f803f80u, dot2(xa.z, 0x3f803f80u, dot2(xa.y, 0x3f803f80u, dot2(xa.x, 0x3f803f80u, 0.f))));
        const float s2 = dot2(xa.w, xa.w, dot2(xa.z, xa.z, dot2(xa.y, xa.y, dot2(xa.x, xa.x, 0.f))));
        const float t = x16_sum(sum16(mix32(s1, s2))) * (1.0f / 512.0f);
        const float mean = __builtin_bit_cast(float, __builtin_amdgcn_readlane(__builtin_bit_cast(int, t), 0));
        const float msq = __builtin_bit_cast(float, __builtin_amdgcn_readlane(__builtin_bit_cast(int, t), 32));
        const float var = vmax(msq - mean * mean, 0.f);
        const float mr = rb(mean), den = rb(__builtin_amdgcn_sqrtf(rb(rb(var) + 1e-5f)));
        const float ln = __fdiv_rn(rb(bf_lo(xv) - mr), den);
        lds_st<u16>(L_LN + (tid >> 7) * 320 + (tid & 127) * 2, (u16)cvtpk(ln, ln));
    } else if (w == 4) {
        lds_st<unsigned>(L_Z + l * 4, 0u);
        lds_st<unsigned>(L_ONE + l * 4, l < 32 ? 0x3f803f80u : 0xff800000u);
    }
    asm volatile("s_cmp_lg_u32 %1, 0\n\ts_cbranch_scc1 1f\n\ts_waitcnt vmcnt(0)\n1:\n\ts_waitcnt vmcnt(6) lgkmcnt(0)\n\ts_barrier"
                 : "+s"(ev) : "s"(u >> 4) : "memory", "scc");
    ST(1);
    k.tag = ev + 1u;
    k.tagv = k.tag;
    PIN(k.tagv);
    if (w < 4) {
        const v4f zz = {0.f, 0.f, 0.f, 0.f};
        v4u bw[4], al[4];
#pragma unroll
        for (int ks = 0; ks < 4; ++ks) { bw[ks] = frag_tr(qa + ks * 4096, qa + ks * 4096 + 512); al[ks] = lds_ld<v4u>(la + ks * 64); }
        v4f acc = mfma(al[0], bw[0], zz);
#pragma unroll
        for (int ks = 1; ks < 4; ++ks) acc = mfma(al[ks], bw[ks], acc);
        put8(k.s_ws, qoff, __float_as_uint(acc.x), k.tagv);
    }
    __builtin_amdgcn_s_barrier();
    ST(2);

    if (att) {
        const unsigned vvo = (unsigned)gq * 256u + (unsigned)((c ^ (gq << 1)) * 16);
#pragma unroll
        for (int i = 6; i < 8; ++i) dma<NTA>(L_A + (w * 8 + i) * 1024, kvo ^ (unsigned)((i & 3) * 64), s_k, kvb + i * 1024);
#pragma unroll
        for (int i = 0; i < NV1; ++i) dma<NTA>(L_B + (w * 8 + i) * 1024, vvo ^ (unsigned)((i & 1) * 128), s_v, kvb + i * 1024);
        unsigned qo = w < 4 ? WS_Q + (unsigned)(gA * 2 + bA) * 16384u + (unsigned)(tid & 255) * 16u : OOB;
        unsigned qo2 = qo + 8192u;
        PIN(qo); PIN(qo2);
        WAITV(NV1);
        ST(3);
        v4u g0, g1, g2, g3;
        const unsigned k4 = 4096u;
        asm volatile("buffer_load_dwordx4 %0, %4, %6, 0 offen sc1\n\t"
                     "buffer_load_dwordx4 %1, %4, %6, %7 offen sc1\n\t"
                     "buffer_load_dwordx4 %2, %5, %6, 0 offen sc1\n\t"
                     "buffer_load_dwordx4 %3, %5, %6, %7 offen sc1"
                     : "=&{v[160:163]}"(g0), "=&{v[164:167]}"(g1), "=&{v[168:171]}"(g2), "=&{v[172:175]}"(g3)
                     : "v"(qo), "v"(qo2), "s"(k.s_ws), "s"(k4) : "memory");
#pragma unroll
        for (int i = NV1; i < 8; ++i) dma<NTA>(L_B + (w * 8 + i) * 1024, vvo ^ (unsigned)((i & 1) * 128), s_v, kvb + i * 1024);
        v4u kf[2][4];
#pragma unroll
        for (int T = 0; T < 2; ++T)
#pragma unroll
            for (int st = 0; st < 4; ++st)
                kf[T][st] = lds_ld<v4u>(L_A + (unsigned)(w * 32 + T * 16 + c) * 256u + (unsigned)(((st * 4 + gq) ^ c) * 16));
        unsigned qw = L_QI + (unsigned)w * 320u + (unsigned)l * 4u;
        unsigned aq0 = c < 8 ? L_QI + (unsigned)(c & 3) * 320u + (unsigned)gq * 16u : L_Z;
        unsigned aq1 = c < 8 ? L_Z : L_QI + (unsigned)(c & 3) * 320u + (unsigned)gq * 16u;
        unsigned mxa = L_MX + (unsigned)(c & 3) * 16u;
        unsigned mxw = mxa + (unsigned)(c >> 2) * 4u;
        unsigned pr = L_P + (unsigned)gq * 64u + (unsigned)(c & 3) * 16u;
        unsigned pw = pr + (unsigned)w * 256u + (unsigned)(c >> 2) * 4u;
        const unsigned vrow = (unsigned)gq * 4u + k.q4;
        unsigned va = L_B + vrow * 256u + k.p4 * 8u + (((unsigned)w ^ (vrow & 7u)) * 32u);
        unsigned oo = l < 16 ? WS_O + (unsigned)(bA * 8 + gA) * 16384u + (unsigned)w * 2048u + (unsigned)s * 256u + (unsigned)l * 16u : OOB;
        PIN(qw); PIN(aq0); PIN(aq1); PIN(mxa); PIN(mxw); PIN(pr); PIN(pw); PIN(va); PIN(oo);
        {
            unsigned t, t2, t3, cnt;
            asm volatile("s_cmp_gt_u32 %[w], 3\n\t"
                         "s_cbranch_scc1 2f\n\t"
                         "s_movk_i32 %[cnt], " SPINS "\n\t"
                         "s_waitcnt vmcnt(%[n])\n"
                         "1:\n\t"
                         "v_bitop3_b32 %[t], v161, v163, %[tag] bitop3:0x7e\n\t"
                         "v_bitop3_b32 %[t2], v165, v167, %[tag] bitop3:0x7e\n\t"
                         "v_bitop3_b32 %[t3], v169, v171, %[tag] bitop3:0x7e\n\t"
                         "v_or3_b32 %[t], %[t], %[t2], %[t3]\n\t"
                         "v_bitop3_b32 %[t2], v173, v175, %[tag] bitop3:0x7e\n\t"
                         "v_or_b32 %[t], %[t], %[t2]\n\t"
                         "v_cmp_ne_u32 vcc, 0, %[t]\n\t"
                         "s_cbranch_vccz 3f\n\t"
                         "buffer_load_dwordx4 v[160:163], %[o1], %[srd], 0 offen sc1\n\t"
                         "buffer_load_dwordx4 v[164:167], %[o1], %[srd], %[k4] offen sc1\n\t"
                         "buffer_load_dwordx4 v[168:171], %[o2], %[srd], 0 offen sc1\n\t"
                         "buffer_load_dwordx4 v[172:175], %[o2], %[srd], %[k4] offen sc1\n\t"
                         "s_sub_u32 %[cnt], %[cnt], 1\n\t"
                         "s_waitcnt vmcnt(0)\n\t"
                         "s_cbranch_scc0 1b\n"
                         "3:\n\t"
                         "v_add_f32 %[t], v160, v164\n\t"
                         "v_add_f32 %[t2], v168, v172\n\t"
                         "v_add_f32 %[t3], v162, v166\n\t"
                         "v_add_f32 %[t], %[t], %[t2]\n\t"
                         "v_add_f32 %[t2], v170, v174\n\t"
                         "v_add_f32 %[t3], %[t3], %[t2]\n\t"
                         "v_cvt_pk_bf16_f32 %[t], %[t], %[t3]\n\t"
                         "ds_write_b32 %[qw], %[t]\n"
                         "2:\n\t"
                         "s_waitcnt lgkmcnt(0)\n\t"
                         "s_barrier"
                         : [t] "=&v"(t), [t2] "=&v"(t2), [t3] "=&v"(t3), [cnt] "=&s"(cnt),
                           "+{v[160:163]}"(g0), "+{v[164:167]}"(g1), "+{v[168:171]}"(g2), "+{v[172:175]}"(g3)
                         : [o1] "v"(qo), [o2] "v"(qo2), [srd] "s"(k.s_ws), [k4] "s"(k4), [tag] "s"(k.tag), [w] "s"(w), [n] "n"(8 - NV1), [qw] "v"(qw)
                         : "memory", "vcc", "scc");
        }
        ST(4);
        float s0, s1;
        {
            v4u qf[2][4];
#pragma unroll
            for (int st = 0; st < 4; ++st) { qf[0][st] = lds_ld<v4u>(aq0 + st * 64); qf[1][st] = lds_ld<v4u>(aq1 + st * 64); }
            __builtin_amdgcn_sched_barrier(0);
            v4f sc = {0.f, 0.f, 0.f, 0.f};
#pragma unroll
            for (int st = 0; st < 4; ++st) { sc = mfma(kf[0][st], qf[0][st], sc); sc = mfma(kf[1][st], qf[1][st], sc); }
            const bool half = (c >> 2) & 1;
            const unsigned r1 = cvtpk(half ? sc.z : sc.x, half ? sc.w : sc.y);
            const unsigned r2 = cvtpk(bf_lo(r1) * 0.08838834764831845f, bf_hi(r1) * 0.08838834764831845f);
            s0 = bf_lo(r2); s1 = bf_hi(r2);
            __builtin_amdgcn_ds_fmaxf((LAS float*)(size_t)mxw, vmax(s0, s1), 0, 0, false);
        }
        BAR();
        float mx;
        {
            const v4f m4 = lds_ld<v4f>(mxa);
            mx = vmax3(m4.x, m4.y, vmax(m4.z, m4.w));
            const float e0 = __builtin_amdgcn_exp2f((s0 - mx) * 1.4426950408889634f), e1 = __builtin_amdgcn_exp2f((s1 - mx) * 1.4426950408889634f);
            lds_st<unsigned>(pw, cvtpk(e0, e1));
        }
        BAR();
        v4u pf[8];
#pragma unroll
        for (int j = 0; j < 8; ++j) pf[j] = lds_ld<v4u>(pr + j * 256);
        if (w == 7) {
            const v4u ones = {0x3f803f80u, 0x3f803f80u, 0x3f803f80u, 0x3f803f80u};
            v4f ls = {0.f, 0.f, 0.f, 0.f};
#pragma unroll
            for (int j = 0; j < 8; ++j) ls = mfma(ones, pf[j], ls);
            put16(k.s_ws, l < 8 ? WS_ML + (unsigned)((bA * 8 + gA) * 8 + s) * 128u + (unsigned)l * 16u : OOB, __float_as_uint(mx), __float_as_uint(ls.x), k.tagv);
        }
        ST(12);
        __builtin_amdgcn_s_barrier();
        if (s < 7) { mlp_load1(k, L_A, fb, w1); WAITV(4); } else { WAITV(0); }
        asm volatile("s_barrier" ::"{v[160:163]}"(g0), "{v[164:167]}"(g1), "{v[168:171]}"(g2), "{v[172:175]}"(g3) : "memory");
        ST(5);
        {
            v4u vf[8];
#pragma unroll
            for (int j = 0; j < 8; ++j) vf[j] = frag_tr(va + j * 8192, va + j * 8192 + 4096);
            __builtin_amdgcn_sched_barrier(0);
            v4f o = {0.f, 0.f, 0.f, 0.f};
#pragma unroll
            for (int j = 0; j < 8; ++j) o = mfma(pf[j], vf[j], o);
            put16(k.s_ws, oo, cvtpk(o.x, o.y), cvtpk(o.z, o.w), k.tagv);
        }
        ST(6);
        if (s < 7) {
            __builtin_amdgcn_s_barrier();
            mlp_load2(k, L_A, fb, w2);
            Mlp m;
            mlp_prep(k, L_A, bM, m);
            mlp_run(STPASS k, bM, fb, m);
        }
    } else {
        if (WOD) __builtin_amdgcn_s_sleep(WOD);
        {
            const v4i s_wo = mksrd(wo, 4u << 20);
            const unsigned vo = (unsigned)(l >> 3) * 1024u + k.p8;
            const unsigned so = (unsigned)(gP * 512 + w * 16) * 1024u + (unsigned)s * 128u;
#pragma unroll
            for (int i = 0; i < 8; ++i) dma<NTO>(L_A + (w * 8 + i) * 1024, vo ^ (unsigned)((i & 1) * 64), s_wo, so + ((i >> 1) * 128 + (i & 1) * 8) * 1024);
        }
        WAITV(0);
        ST(3);
        v4u wf[4][2];
        {
            const unsigned a = L_A + (unsigned)w * 8192u + k.tr128;
#pragma unroll
            for (int t = 0; t < 4; ++t)
#pragma unroll
                for (int ks = 0; ks < 2; ++ks) { const unsigned q = a + (((unsigned)t ^ k.trsw) * 32u) + ks * 4096; wf[t][ks] = frag_tr(q, q + 512); }
        }
        if (w == 7) {
            v4u ml;
            poll1(ml, WS_ML + (unsigned)(bP * 8 + gP) * 1024u + (unsigned)l * 16u, k.s_ws, k.tag);
            const float m = __uint_as_float(ml.x);
            const float M = x32_max(x16_max(vmax(m, dppf<XOR8>(m))));
            const float f = __builtin_amdgcn_exp2f((m - M) * 1.4426950408889634f);
            float L = f * __uint_as_float(ml.z);
            L += dppf<XOR8>(L);
            L = x32_sum(x16_sum(L));
            lds_st<float>(L_W + (unsigned)(l >> 3) * 16u + (unsigned)(l & 3) * 4u, f / L);
        }
        unsigned oo = WS_O + (unsigned)(bP * 8 + gP) * 16384u + (unsigned)w * 2048u + (unsigned)l * 16u;
        unsigned ow = L_OS + (unsigned)w * 128u + (unsigned)l * 2u;
        unsigned ar[4];
#pragma unroll
        for (int t = 0; t < 4; ++t) { ar[t] = (c & 3) == t ? L_OS + (unsigned)w * 128u + (unsigned)gq * 16u : L_Z; PIN(ar[t]); }
        unsigned hw = L_HR + (unsigned)w * 256u + (unsigned)c * 16u;
        unsigned hr = L_HR + (unsigned)gq * 512u + (unsigned)c * 16u;
        unsigned hoff = (gq & 1) ? OOB : WS_H + (unsigned)((bP * 8 + s) * 8 + gP) * 256u + (unsigned)(gq >> 1) * 128u + (unsigned)c * 8u;
        PIN(oo); PIN(ow); PIN(hw); PIN(hr); PIN(hoff);
        BAR();
        v4f wa = lds_ld<v4f>(L_W + (unsigned)gq * 16u), wb = lds_ld<v4f>(L_W + (unsigned)gq * 16u + 64u);
        PIN(wa); PIN(wb);
        ST(4);
        {
            v4u o0, o1;
            poll2(o0, o1, oo, k.s_ws, k.tag);
            ST(5);
            const float v0 = bf_lo(o0.x) * wa.x + bf_lo(o1.x) * wb.x, v1 = bf_hi(o0.x) * wa.y + bf_hi(o1.x) * wb.y;
            const float v2 = bf_lo(o0.z) * wa.z + bf_lo(o1.z) * wb.z, v3 = bf_hi(o0.z) * wa.w + bf_hi(o1.z) * wb.w;
            const float oc = mix16(mix32(v0, v2), mix32(v1, v3));
            lds_st<u16>(ow, (u16)cvtpk(oc, oc));
            v4u af[4][2];
#pragma unroll
            for (int t = 0; t < 4; ++t) { af[t][0] = lds_ld<v4u>(ar[t]); af[t][1] = lds_ld<v4u>(ar[t] + 64); }
            __builtin_amdgcn_sched_barrier(0);
            v4f acc = {0.f, 0.f, 0.f, 0.f};
#pragma unroll
            for (int t = 0; t < 4; ++t) { acc = mfma(af[t][0], wf[t][0], acc); acc = mfma(af[t][1], wf[t][1], acc); }
            lds_st<v4f>(hw, acc);
        }
        BAR();
        if (w == 0) {
            const v4f t = lds_ld<v4f>(hr) + lds_ld<v4f>(hr + 256);
            const float a = x16_sum(mix32(t.x, t.z)), bq = x16_sum(mix32(t.y, t.w));
            put8(k.s_ws, hoff, cvtpk(a, bq), k.tagv);
            ST(6);
        }
        if (w == 4 && s < 4) fin_run(STPASS k, bF, cF, s, out);
    }
}

extern "C" void kernel_launch(void* const* d_in, const int* in_sizes, int n_in, void* d_out, int out_size, void* d_ws, size_t ws_size,
                              hipStream_t stream) {
    fused_block<<<256, 512, 0, stream>>>((const u16*)d_in[0], (const u16*)d_in[1], (const u16*)d_in[2], (const u16*)d_in[3], (const u16*)d_in[4],
                                         (const u16*)d_in[6], (const u16*)d_in[7], (u16*)d_out, (unsigned char*)d_ws);
}
```

```cpp
#include <hip/hip_runtime.h>
#include <hip/hip_bf16.h>

typedef unsigned short u16;
typedef unsigned long long u64;
typedef unsigned v4u __attribute__((ext_vector_type(4)));
typedef unsigned v2u __attribute__((ext_vector_type(2)));
typedef int v4i __attribute__((ext_vector_type(4)));
typedef float v4f __attribute__((ext_vector_type(4)));
typedef float v2f __attribute__((ext_vector_type(2)));
typedef short v4s __attribute__((ext_vector_type(4)));
typedef __bf16 v8bf __attribute__((ext_vector_type(8)));
typedef __bf16 v2bf __attribute__((ext_vector_type(2)));
#define LAS __attribute__((address_space(3)))
#define DEV __device__ __forceinline__

#define REP 4
#define REM 4
#define MD 0
#define PW 0
#define QD 0
#define TLBW 1
#define NTA 1
#define NTW 1
#define HD 0

constexpr unsigned WS_Q = 4096;
constexpr unsigned WS_ML = WS_Q + 262144;
constexpr unsigned WS_O = WS_ML + 32768;
constexpr unsigned WS_H = WS_O + 524288;
constexpr unsigned WS_Y = WS_H + 32768;
constexpr unsigned WS_END = WS_Y + 458752;

constexpr unsigned L_K = 0;
constexpr unsigned L_V = 32768;
constexpr unsigned L_R = 65536;
constexpr unsigned L_C = 131072;
constexpr unsigned L_LN = 147456;
constexpr unsigned L_QI = L_LN + 640;
constexpr unsigned L_Z = L_QI + 1280;
constexpr unsigned L_ONE = L_Z + 256;
constexpr unsigned L_MX = L_ONE + 128;
constexpr unsigned L_W = L_MX + 64;
constexpr unsigned L_P = L_W + 320;
constexpr unsigned L_OS = L_P + 1024;
constexpr unsigned L_HR = L_OS + 1024;
constexpr unsigned L_ZR = L_HR + 2048;
constexpr unsigned L_GS = L_ZR + 2560;
constexpr unsigned L_ZE = L_GS + 128;
constexpr unsigned L_DM = L_ZE + 2048;
constexpr unsigned L_END = L_DM + 1536;

DEV float bf_lo(unsigned v) { return __uint_as_float(v << 16); }
DEV float bf_hi(unsigned v) { return __uint_as_float(v & 0xffff0000u); }
DEV unsigned cvtpk(float a, float b) { v2f t = {a, b}; return __builtin_bit_cast(unsigned, __builtin_convertvector(t, v2bf)); }
DEV float rb(float a) { return bf_lo(cvtpk(a, a)); }
DEV float vmax(float a, float b) { return __builtin_elementwise_maximum(a, b); }
DEV float vmax3(float a, float b, float c) { return vmax(a, vmax(b, c)); }
template <int CTRL> DEV float dppf(float x) { return __builtin_bit_cast(float, __builtin_amdgcn_mov_dpp(__builtin_bit_cast(int, x), CTRL, 0xf, 0xf, true)); }
template <int CTRL> DEV unsigned dppu(unsigned x) { return (unsigned)__builtin_amdgcn_mov_dpp((int)x, CTRL, 0xf, 0xf, true); }
constexpr int XOR1 = 0xB1, XOR2 = 0x4E, XOR7 = 0x141, XOR8 = 0x128;
DEV float mix16(float a, float b) { auto s = __builtin_amdgcn_permlane16_swap(__float_as_uint(a), __float_as_uint(b), false, false); return __uint_as_float(s[0]) + __uint_as_float(s[1]); }
DEV float mix32(float a, float b) { auto s = __builtin_amdgcn_permlane32_swap(__float_as_uint(a), __float_as_uint(b), false, false); return __uint_as_float(s[0]) + __uint_as_float(s[1]); }
DEV float x16_sum(float x) { return mix16(x, x); }
DEV float x32_sum(float x) { return mix32(x, x); }
DEV float x16_max(float x) { auto s = __builtin_amdgcn_permlane16_swap(__float_as_uint(x), __float_as_uint(x), false, false); return vmax(__uint_as_float(s[0]), __uint_as_float(s[1])); }
DEV float x32_max(float x) { auto s = __builtin_amdgcn_permlane32_swap(__float_as_uint(x), __float_as_uint(x), false, false); return vmax(__uint_as_float(s[0]), __uint_as_float(s[1])); }
DEV float sum8(float x) { x += dppf<XOR1>(x); x += dppf<XOR2>(x); x += dppf<XOR7>(x); return x; }
DEV float sum16(float x) { x = sum8(x); x += dppf<XOR8>(x); return x; }
DEV float dot2(unsigned a, unsigned b, float c) { return __builtin_amdgcn_fdot2_f32_bf16(__builtin_bit_cast(v2bf, a), __builtin_bit_cast(v2bf, b), c, false); }
DEV v4f mfma(v4u a, v4u b, v4f c) { return __builtin_amdgcn_mfma_f32_16x16x32_bf16(__builtin_bit_cast(v8bf, a), __builtin_bit_cast(v8bf, b), c, 0, 0, 0); }
DEV v4i mksrd(const void* p, unsigned n) { u64 a = (u64)p; v4i r; r.x = (int)(unsigned)a; r.y = (int)(unsigned)(a >> 32); r.z = (int)n; r.w = 0x00020000; return r; }

template <typename T> DEV T lds_ld(unsigned a) { return *(LAS T*)(size_t)a; }
template <typename T> DEV void lds_st(unsigned a, T v) { *(LAS T*)(size_t)a = v; }
DEV v2u lds_tr(unsigned a) { return __builtin_bit_cast(v2u, __builtin_amdgcn_ds_read_tr16_b64_v4i16((LAS v4s*)(size_t)a)); }
DEV v4u frag_tr(unsigned a0, unsigned a1) { v2u p = lds_tr(a0), q = lds_tr(a1); v4u r = {p.x, p.y, q.x, q.y}; return r; }
#define PIN(x) asm volatile("" : "+v"(x))

template <int NT> DEV void dma(unsigned ldsdst, unsigned voff, v4i srd, unsigned soff) {
    if (NT) asm volatile("s_mov_b32 m0, %0\n\ts_nop 0\n\tbuffer_load_dwordx4 %1, %2, %3 offen nt lds" ::"s"(ldsdst), "v"(voff), "s"(srd), "s"(soff) : "memory");
    else asm volatile("s_mov_b32 m0, %0\n\ts_nop 0\n\tbuffer_load_dwordx4 %1, %2, %3 offen lds" ::"s"(ldsdst), "v"(voff), "s"(srd), "s"(soff) : "memory");
}
#define WAITV(n) asm volatile("s_waitcnt vmcnt(%0)" ::"n"(n) : "memory")
#define BAR() asm volatile("s_waitcnt lgkmcnt(0)\n\ts_barrier" ::: "memory")
constexpr unsigned OOB = 0x7ff00000u;
DEV void put16(v4i srd, unsigned voff, unsigned a, unsigned b, unsigned tagv) {
    v4u v = {a, tagv, b, tagv};
    asm volatile("buffer_store_dwordx4 %0, %1, %2, 0 offen sc1\n\ts_nop 1" ::"v"(v), "v"(voff), "s"(srd) : "memory");
}
DEV void put8(v4i srd, unsigned voff, unsigned a, unsigned tagv) {
    v2u v = {a, tagv};
    asm volatile("buffer_store_dwordx2 %0, %1, %2, 0 offen sc1\n\ts_nop 1" ::"v"(v), "v"(voff), "s"(srd) : "memory");
}

#define SPINS "0x3000"
DEV void poll1(v4u& r0, unsigned off, v4i srd, unsigned tag) {
    unsigned t, cnt;
    asm volatile("s_movk_i32 %[cnt], " SPINS "\n"
                 "1:\n\t"
                 "buffer_load_dwordx4 v[160:163], %[off], %[srd], 0 offen sc1\n\t"
                 "s_sub_u32 %[cnt], %[cnt], 1\n\t"
                 "s_waitcnt vmcnt(0)\n\t"
                 "s_cbranch_scc1 2f\n\t"
                 "v_bitop3_b32 %[t], v161, v163, %[tag] bitop3:0x7e\n\t"
                 "v_cmp_ne_u32 vcc, 0, %[t]\n\t"
                 "s_cbranch_vccnz 1b\n"
                 "2:"
                 : [t] "=&v"(t), [cnt] "=&s"(cnt), "=&{v[160:163]}"(r0) : [off] "v"(off), [srd] "s"(srd), [tag] "s"(tag) : "memory", "vcc", "scc");
}
DEV void poll4(v4u& r0, v4u& r1, v4u& r2, v4u& r3, unsigned off, v4i srd, unsigned tag) {
    unsigned t, t2, t3, cnt;
    asm volatile("s_movk_i32 %[cnt], " SPINS "\n"
                 "1:\n\t"
                 "buffer_load_dwordx4 v[160:163], %[off], %[srd], 0 offen sc1\n\t"
                 "buffer_load_dwordx4 v[164:167], %[off], %[srd], 0 offen offset:1024 sc1\n\t"
                 "buffer_load_dwordx4 v[168:171], %[off], %[srd], 0 offen offset:2048 sc1\n\t"
                 "buffer_load_dwordx4 v[172:175], %[off], %[srd], 0 offen offset:3072 sc1\n\t"
                 "s_sub_u32 %[cnt], %[cnt], 1\n\t"
                 "s_waitcnt vmcnt(2)\n\t"
                 "v_bitop3_b32 %[t], v161, v163, %[tag] bitop3:0x7e\n\t"
                 "v_bitop3_b32 %[t2], v165, v167, %[tag] bitop3:0x7e\n\t"
                 "s_waitcnt vmcnt(0)\n\t"
                 "s_cbranch_scc1 2f\n\t"
                 "v_bitop3_b32 %[t3], v169, v171, %[tag] bitop3:0x7e\n\t"
                 "v_or3_b32 %[t], %[t], %[t2], %[t3]\n\t"
                 "v_bitop3_b32 %[t2], v173, v175, %[tag] bitop3:0x7e\n\t"
                 "v_or_b32 %[t], %[t], %[t2]\n\t"
                 "v_cmp_ne_u32 vcc, 0, %[t]\n\t"
                 "s_cbranch_vccnz 1b\n"
                 "2:"
                 : [t] "=&v"(t), [t2] "=&v"(t2), [t3] "=&v"(t3), [cnt] "=&s"(cnt), "=&{v[160:163]}"(r0), "=&{v[164:167]}"(r1), "=&{v[168:171]}"(r2), "=&{v[172:175]}"(r3)
                 : [off] "v"(off), [srd] "s"(srd), [tag] "s"(tag) : "memory", "vcc", "scc");
}
DEV void poll2_lds(unsigned off, v4i srd, unsigned tag, unsigned la) {
    unsigned t, t2, cnt;
    asm volatile("s_movk_i32 %[cnt], " SPINS "\n"
                 "1:\n\t"
                 "buffer_load_dwordx4 v[160:163], %[off], %[srd], 0 offen sc1\n\t"
                 "buffer_load_dwordx4 v[164:167], %[off], %[srd], 0 offen offset:1024 sc1\n\t"
                 "s_sub_u32 %[cnt], %[cnt], 1\n\t"
                 "s_waitcnt vmcnt(0)\n\t"
                 "ds_write2_b32 %[la], v160, v162 offset1:1\n\t"
                 "ds_write2_b32 %[la], v164, v166 offset0:160 offset1:161\n\t"
                 "s_cbranch_scc1 2f\n\t"
                 "v_bitop3_b32 %[t], v161, v163, %[tag] bitop3:0x7e\n\t"
                 "v_bitop3_b32 %[t2], v165, v167, %[tag] bitop3:0x7e\n\t"
                 "v_or_b32 %[t], %[t], %[t2]\n\t"
                 "v_cmp_ne_u32 vcc, 0, %[t]\n\t"
                 "s_cbranch_vccz 2f\n\t"
                 "s_waitcnt lgkmcnt(0)\n\t"
                 "s_branch 1b\n"
                 "2:"
                 : [t] "=&v"(t), [t2] "=&v"(t2), [cnt] "=&s"(cnt)
                 : [off] "v"(off), [srd] "s"(srd), [tag] "s"(tag), [la] "v"(la)
                 : "memory", "vcc", "scc", "v160", "v161", "v162", "v163", "v164", "v165", "v166", "v167");
}

#define ST(i) do { } while (0)
#define STARG
#define STPASS

struct Ctx {
    int l, w, gq, c;
    unsigned q4, p4, p8, trsw, tr128;
    unsigned tag, tagv;
    v4i s_ws, s_x;
};

template <int I0, int I1> DEV void mlp_load(const Ctx& k, int fb, const u16* w1, const u16* w2) {
    const v4i s_w1 = mksrd(w1, 1835008u), s_w2 = mksrd(w2, 1835008u);
    const unsigned vo = (unsigned)(k.l >> 2) * 3584u + (unsigned)(((k.l & 3) ^ ((k.l >> 5) << 1)) * 16);
    const unsigned so = (unsigned)(k.w * 64) * 3584u + (unsigned)fb * 64u;
    const unsigned vo2 = (unsigned)(k.l >> 3) * 1024u + k.p8;
    const unsigned so2 = (unsigned)(fb * 32) * 1024u + (unsigned)k.w * 128u;
#pragma unroll
    for (int i = I0; i < I1; ++i) {
        if (i < 4) dma<NTW>(L_R + k.w * 8192 + i * 1024, vo, s_w1, so + i * 16 * 3584);
        else dma<NTW>(L_R + k.w * 8192 + i * 1024, vo2 ^ (unsigned)((i & 1) * 64), s_w2, so2 + (i - 4) * 8192);
    }
}
template <int I0, int I1> DEV void prj_load(const Ctx& k, int gP, int cb, const u16* wo) {
    const v4i s_wo = mksrd(wo, 4u << 20);
    const unsigned vo = (unsigned)(k.l >> 3) * 1024u + k.p8;
    const unsigned so = (unsigned)(gP * 512 + k.w * 16) * 1024u + (unsigned)cb * 128u;
#pragma unroll
    for (int i = I0; i < I1; ++i) dma<NTW>(L_R + (k.w * 8 + i) * 1024, vo ^ (unsigned)((i & 1) * 64), s_wo, so + ((i >> 1) * 128 + (i & 1) * 8) * 1024);
}
struct Mlp { v4u f1[2][2], f2[4]; float xw, cs; unsigned ha; };
DEV void mlp_prep(STARG const Ctx& k, unsigned MB, int bM, Mlp& m) {
    const int l = k.l, w = k.w, gq = k.gq, c = k.c;
    const unsigned hs = L_C + (unsigned)w * 1440u;
    {
        unsigned x0, x1;
        const unsigned xo = (unsigned)(bM * 512 + w * 64 + ((l >> 4) & 1) * 32 + c) * 2u;
        asm volatile("buffer_load_ushort %0, %2, %3, 0 offen\n\tbuffer_load_ushort %1, %2, %3, 0 offen offset:32\n\ts_waitcnt vmcnt(0)"
                     : "=&v"(x0), "=&v"(x1) : "v"(xo), "s"(k.s_x) : "memory");
        ST(13);
        lds_st<unsigned>(hs + 8 * 160 + (l & 31) * 4, x0 | (x1 << 16));
    }
    {
        const unsigned a = MB + (unsigned)w * 8192u + ((k.q4 & 1) * 16 + (unsigned)gq * 4 + (k.q4 >> 1)) * 64u + k.p4 * 8u;
        const unsigned sx = (unsigned)(gq >> 1) * 32u;
#pragma unroll
        for (int ks = 0; ks < 2; ++ks) {
            m.f1[ks][0] = frag_tr(a + sx + ks * 2048, a + sx + ks * 2048 + 128);
            m.f1[ks][1] = frag_tr(a + (sx ^ 32u) + ks * 2048, a + (sx ^ 32u) + ks * 2048 + 128);
        }
        const unsigned a2 = MB + (unsigned)w * 8192u + 4096u + k.tr128;
#pragma unroll
        for (int nt = 0; nt < 4; ++nt) { const unsigned q = a2 + (((unsigned)nt ^ k.trsw) * 32u); m.f2[nt] = frag_tr(q, q + 512); }
    }
    m.ha = (c <= 8 ? hs + (unsigned)c * 160u : (c == 12 ? L_ONE : L_Z)) + (unsigned)gq * 16u;
    v4f d0 = {0.f, 0.f, 0.f, 0.f}, d1 = {0.f, 0.f, 0.f, 0.f};
#pragma unroll
    for (int ks = 0; ks < 2; ++ks) { const v4u xf = lds_ld<v4u>(m.ha + ks * 64); d0 = mfma(xf, m.f1[ks][0], d0); d1 = mfma(xf, m.f1[ks][1], d1); }
    if (gq >= 2) { const unsigned a = L_ZE + (unsigned)(w * 2 + (gq & 1)) * 128u + (unsigned)c * 4u; lds_st<float>(a, d0.x); lds_st<float>(a + 64, d1.x); }
    BAR();
    const unsigned a = L_ZE + (unsigned)(c >> 3) * 1024u + (unsigned)(gq * 8 + (c & 7)) * 4u;
    float xw = (lds_ld<float>(a) + lds_ld<float>(a + 256)) + (lds_ld<float>(a + 512) + lds_ld<float>(a + 768));
    float cs = (lds_ld<float>(a + 128) + lds_ld<float>(a + 384)) + (lds_ld<float>(a + 640) + lds_ld<float>(a + 896));
    m.xw = xw + dppf<XOR8>(xw); m.cs = cs + dppf<XOR8>(cs);
}
DEV void mlp_run(STARG const Ctx& k, int bM, int fb, Mlp& m) {
    const int l = k.l, w = k.w, gq = k.gq, c = k.c;
    unsigned ho = WS_H + (unsigned)(bM * 8 + w) * 2048u + (unsigned)l * 16u;
    unsigned sa = L_C + (unsigned)w * 1440u + (unsigned)gq * 160u + (unsigned)c * 8u;
    unsigned za = gq < 2 ? L_ZR + (unsigned)c * 80u + (unsigned)(w * 2 + gq) * 4u : L_DM + (unsigned)(l & 31) * 4u;
    unsigned ga = L_GS + (unsigned)(w * 16 + gq * 4);
    unsigned zr = L_ZR + (unsigned)(gq * 8 + (c & 7)) * 80u + (unsigned)(c >> 3) * 32u;
    unsigned gr = L_GS + (unsigned)(l & 7) * 16u;
    const unsigned rblk = (unsigned)(bM * 32 + w * 4 + (gq >> 1) * 2 + (c >> 3));
    unsigned yo = (gq & 1) ? OOB : WS_Y + (rblk * 56u + (unsigned)fb) * 128u + (unsigned)(c & 7) * 16u;
    float gmask = c == 12 ? 0.f : 1.f;
    PIN(ho); PIN(sa); PIN(za); PIN(ga); PIN(zr); PIN(gr); PIN(yo); PIN(gmask); PIN(m.xw); PIN(m.cs); PIN(m.ha);
    if (HD) __builtin_amdgcn_s_sleep(HD);
    ST(7);
    poll2_lds(ho, k.s_ws, k.tag, sa);
    ST(8);
    {
        const v4f zz = {0.f, 0.f, 0.f, 0.f};
        const v4u x0 = lds_ld<v4u>(m.ha), x1 = lds_ld<v4u>(m.ha + 64);
        v4f gm = mfma(x0, x0, zz);
        gm = mfma(x1, x1, gm);
        v4f d0 = mfma(x0, m.f1[0][0], zz), d1 = mfma(x0, m.f1[0][1], zz);
        d0 = mfma(x1, m.f1[1][0], d0); d1 = mfma(x1, m.f1[1][1], d1);
        const float gs = sum16(((gm.x + gm.y) + (gm.z + gm.w)) * gmask);
        lds_st<float>(ga, gs);
        lds_st<float>(za, (d0.x + d0.y) + (d0.z + d0.w));
        lds_st<float>(za + 1280, (d1.x + d1.y) + (d1.z + d1.w));
    }
    BAR();
    {
        const v4f g4 = lds_ld<v4f>(gr);
        const v4f za4 = lds_ld<v4f>(zr), zb4 = lds_ld<v4f>(zr + 16);
        const float s2 = sum8((g4.x + g4.y) + g4.z) * (1.0f / 512.0f), mean = sum8(g4.w) * (1.0f / 512.0f);
        const v4f zs = za4 + zb4;
        float z = (zs.x + zs.y) + (zs.z + zs.w);
        z += dppf<XOR8>(z);
        const float var = vmax(s2 - mean * mean, 0.f);
        const float mr = rb(mean), den = rb(__builtin_amdgcn_sqrtf(rb(rb(var) + 1e-5f)));
        z = rb(((z + m.xw) - mr * m.cs) * __builtin_amdgcn_rcpf(den));
        const float hv = z * __builtin_amdgcn_rcpf(1.0f + __builtin_amdgcn_exp2f(z * -1.4426950408889634f));
        const unsigned pk = cvtpk(hv, dppf<0x101>(hv));
        const v4u af = {pk, dppu<0x102>(pk), dppu<0x104>(pk), dppu<0x106>(pk)};
        const v4f zz = {0.f, 0.f, 0.f, 0.f};
        const v4f y0 = mfma(af, m.f2[0], zz), y2 = mfma(af, m.f2[2], zz), y1 = mfma(af, m.f2[1], zz), y3 = mfma(af, m.f2[3], zz);
        const bool hi = gq >= 2;
        put16(k.s_ws, yo, __float_as_uint(hi ? y2.x : y0.x), __float_as_uint(hi ? y3.x : y1.x), k.tagv);
    }
    ST(9);
}
DEV void fin_run(STARG const Ctx& k, int bF, int cF, int s, u16* out) {
    const int l = k.l, n7 = l & 7;
    const int col = cF * 64 + (s >> 1) * 32 + (s & 1) * 8 + n7;
    unsigned xr0, xr1;
    const unsigned xo = (unsigned)(bF * 512 + col) * 2u;
    asm volatile("buffer_load_ushort %0, %2, %3, 0 offen\n\tbuffer_load_ushort %1, %2, %3, 0 offen offset:32\n\ts_waitcnt vmcnt(0)"
                 : "=&v"(xr0), "=&v"(xr1) : "v"(xo), "s"(k.s_x) : "memory");
    v4u h0;
    poll1(h0, WS_H + (unsigned)((bF * 8 + cF) * 8 + (l >> 3)) * 256u + (unsigned)(s * 4 + (n7 >> 1)) * 16u, k.s_ws, k.tag);
    const unsigned d0 = (n7 & 1) ? h0.z : h0.x;
    float a = mix32(bf_lo(d0), bf_hi(d0));
    a += dppf<XOR8>(a);
    a = x16_sum(a);
    float hh = rb(bf_lo(l < 32 ? xr0 : xr1) + rb(a));
    unsigned yo = WS_Y + (unsigned)(bF * 32 + cF * 4 + s) * 7168u + (unsigned)l * 16u, yo2 = yo + 4096u;
    const v4i s_out = mksrd(out, 2048);
    unsigned oo = (l & 24) ? OOB : xo + (unsigned)(l >> 5) * 32u;
    PIN(hh); PIN(yo); PIN(yo2); PIN(oo);
    ST(7);
    v4u y0, y1, y2, y3, y4, y5, y6;
    unsigned t, t2, t3, cnt;
    asm volatile("s_movk_i32 %[cnt], " SPINS "\n"
                 "1:\n\t"
                 "buffer_load_dwordx4 v[160:163], %[o1], %[srd], 0 offen sc1\n\t"
                 "buffer_load_dwordx4 v[164:167], %[o1], %[srd], 0 offen offset:1024 sc1\n\t"
                 "buffer_load_dwordx4 v[168:171], %[o1], %[srd], 0 offen offset:2048 sc1\n\t"
                 "buffer_load_dwordx4 v[172:175], %[o1], %[srd], 0 offen offset:3072 sc1\n\t"
                 "buffer_load_dwordx4 v[176:179], %[o2], %[srd], 0 offen sc1\n\t"
                 "buffer_load_dwordx4 v[180:183], %[o2], %[srd], 0 offen offset:1024 sc1\n\t"
                 "buffer_load_dwordx4 v[184:187], %[o2], %[srd], 0 offen offset:2048 sc1\n\t"
                 "s_sub_u32 %[cnt], %[cnt], 1\n\t"
                 "s_waitcnt vmcnt(5)\n\t"
                 "v_bitop3_b32 %[t], v161, v163, %[tag] bitop3:0x7e\n\t"
                 "v_bitop3_b32 %[t2], v165, v167, %[tag] bitop3:0x7e\n\t"
                 "s_waitcnt vmcnt(3)\n\t"
                 "v_bitop3_b32 %[t3], v169, v171, %[tag] bitop3:0x7e\n\t"
                 "v_or3_b32 %[t], %[t], %[t2], %[t3]\n\t"
                 "v_bitop3_b32 %[t2], v173, v175, %[tag] bitop3:0x7e\n\t"
                 "s_waitcnt vmcnt(1)\n\t"
                 "v_bitop3_b32 %[t3], v177, v179, %[tag] bitop3:0x7e\n\t"
                 "v_or3_b32 %[t], %[t], %[t2], %[t3]\n\t"
                 "v_bitop3_b32 %[t2], v181, v183, %[tag] bitop3:0x7e\n\t"
                 "s_waitcnt vmcnt(0)\n\t"
                 "s_cbranch_scc1 2f\n\t"
                 "v_bitop3_b32 %[t3], v185, v187, %[tag] bitop3:0x7e\n\t"
                 "v_or3_b32 %[t], %[t], %[t2], %[t3]\n\t"
                 "v_cmp_ne_u32 vcc, 0, %[t]\n\t"
                 "s_cbranch_vccnz 1b\n"
                 "2:"
                 : [t] "=&v"(t), [t2] "=&v"(t2), [t3] "=&v"(t3), [cnt] "=&s"(cnt), "=&{v[160:163]}"(y0), "=&{v[164:167]}"(y1), "=&{v[168:171]}"(y2),
                   "=&{v[172:175]}"(y3), "=&{v[176:179]}"(y4), "=&{v[180:183]}"(y5), "=&{v[184:187]}"(y6)
                 : [o1] "v"(yo), [o2] "v"(yo2), [srd] "s"(k.s_ws), [tag] "s"(k.tag) : "memory", "vcc", "scc");
    ST(8);
#define F_(v) __uint_as_float(v)
    const float ya = ((F_(y0.x) + F_(y1.x)) + (F_(y2.x) + F_(y3.x))) + ((F_(y4.x) + F_(y5.x)) + F_(y6.x));
    const float yb = ((F_(y0.z) + F_(y1.z)) + (F_(y2.z) + F_(y3.z))) + ((F_(y4.z) + F_(y5.z)) + F_(y6.z));
    float y = mix32(ya, yb);
    y += dppf<XOR8>(y);
    y = x16_sum(y);
    const unsigned r = cvtpk(hh + rb(y), 0.f);
    asm volatile("buffer_store_short %0, %1, %2, 0 offen" ::"v"(r), "v"(oo), "s"(s_out) : "memory");
    if ((bF | cF | s) == 0) {
        const unsigned zo = l ? OOB : 0u;
        asm volatile("buffer_store_dword %0, %1, %2, 0 offen sc1" ::"v"(k.tagv), "v"(zo), "s"(k.s_ws) : "memory");
    }
    ST(9);
}

__global__ void __launch_bounds__(512, 2)
fused_block(const u16* __restrict__ x, const u16* __restrict__ kc, const u16* __restrict__ vc, const u16* __restrict__ wq,
            const u16* __restrict__ wo, const u16* __restrict__ w1, const u16* __restrict__ w2, u16* __restrict__ out,
            unsigned char* __restrict__ ws) {
    __shared__ __attribute__((aligned(1024))) unsigned char lds_raw[L_END];
    asm volatile("" ::"v"((LAS unsigned char*)lds_raw));
    const int tid = threadIdx.x, l = tid & 63;
    const int w = __builtin_amdgcn_readfirstlane(tid >> 6);
    const int xc = blockIdx.x & 7, u = blockIdx.x >> 3;
    const int gq = l >> 4, c = l & 15;
    const int kq = u & 3, cq = u >> 2, gQ = (xc + 3) & 7;
    const int bA = u >> 4, gA = (xc + 7) & 7, s = u & 15;
    const bool prj = xc < 4;
    const int gP = xc * 2 + ((u >> 3) & 1), cb = u & 7;
    const int fb = (xc - 4) * 14 + (s < 14 ? s : 13);
    Ctx k;
    k.l = l; k.w = w; k.gq = gq; k.c = c;
    k.s_ws = mksrd(ws, WS_END); k.s_x = mksrd(x, 2048);
    unsigned ev;
    asm volatile("s_load_dword %0, %1, 0x0" : "=s"(ev) : "s"(ws), "s"(kc), "s"(vc), "s"(wq), "s"(wo), "s"(w1), "s"(w2), "s"(out) : "memory");

    unsigned xv; v4u xa;
    {
        const unsigned xo = (unsigned)((w >> 1) & 1) * 1024u + (unsigned)l * 16u, xvo = (unsigned)(((tid >> 7) & 1) * 512 + kq * 128 + (tid & 127)) * 2u;
        asm volatile("buffer_load_dwordx4 %0, %2, %3, 0 offen\n\t"
                     "buffer_load_ushort %1, %4, %3, 0 offen"
                     : "=&{v[160:163]}"(xa), "=&{v164}"(xv) : "v"(xo), "s"(k.s_x), "v"(xvo) : "memory");
    }
    k.p8 = (unsigned)(((l & 7) ^ (((l >> 4) & 1) << 1)) * 16);
    {
        const v4i s_wq = mksrd(wq, 4u << 20);
        const unsigned vo = (unsigned)(l >> 3) * 8192u + k.p8;
        const unsigned so = (unsigned)(kq * 128 + w * 16) * 8192u + (unsigned)(gQ * 512 + cq * 64) * 2u;
        dma<NTA>(L_C + w * 2048, vo, s_wq, so);
        dma<NTA>(L_C + w * 2048 + 1024, vo ^ 64u, s_wq, so + 8u * 8192u);
    }
    const v4i s_k = mksrd(kc, 32u << 20), s_v = mksrd(vc, 32u << 20);
    const unsigned kvb = (unsigned)((bA * 8 + gA) * 8192 + 6144 + s * 128 + w * 16) * 256u;
    unsigned vvo = (unsigned)gq * 256u + (unsigned)((c ^ (gq << 1)) * 16);
    unsigned kvo = (unsigned)gq * 256u + (unsigned)((c ^ gq) * 16);
#define KLD(i) dma<NTA>(L_K + (w * 4 + (i)) * 1024, kvo ^ (unsigned)((i) * 64), s_k, kvb + (i) * 1024)
#define VLD(i) dma<NTA>(L_V + (w * 4 + (i)) * 1024, vvo ^ (unsigned)(((i) & 1) * 128), s_v, kvb + (i) * 1024)
    k.q4 = (unsigned)((l >> 2) & 3); k.p4 = (unsigned)(l & 3);
    k.trsw = (k.q4 >> 1) | ((unsigned)(gq & 1) << 1);
    k.tr128 = (unsigned)(gq * 8 + k.q4) * 128u + k.p4 * 8u;
    unsigned qa = L_C + k.tr128 + (((unsigned)w ^ k.trsw) * 32u);
    unsigned la = L_LN + (unsigned)((c >> 2) & 1) * 320u + (unsigned)gq * 16u;
    unsigned qoff = (l < 32 && w < 4) ? WS_Q + (unsigned)((((gQ * 2 + gq) * 4 + kq) * 512) + cq * 64 + w * 16 + c) * 8u : OOB;
    PIN(qa); PIN(la); PIN(qoff);
    asm volatile("s_waitcnt vmcnt(%2)" : "+{v[160:163]}"(xa), "+{v164}"(xv) : "n"(2) : "memory");
    if (w < 4) {
        const float s1 = dot2(xa.w, 0x3f803f80u, dot2(xa.z, 0x3f803f80u, dot2(xa.y, 0x3f803f80u, dot2(xa.x, 0x3f803f80u, 0.f))));
        const float s2 = dot2(xa.w, xa.w, dot2(xa.z, xa.z, dot2(xa.y, xa.y, dot2(xa.x, xa.x, 0.f))));
        const float t = x16_sum(sum16(mix32(s1, s2))) * (1.0f / 512.0f);
        const float mean = __builtin_bit_cast(float, __builtin_amdgcn_readlane(__builtin_bit_cast(int, t), 0));
        const float msq = __builtin_bit_cast(float, __builtin_amdgcn_readlane(__builtin_bit_cast(int, t), 32));
        const float var = vmax(msq - mean * mean, 0.f);
        const float mr = rb(mean), den = rb(__builtin_amdgcn_sqrtf(rb(rb(var) + 1e-5f)));
        const float ln = __fdiv_rn(rb(bf_lo(xv) - mr), den);
        lds_st<u16>(L_LN + (tid >> 7) * 320 + (tid & 127) * 2, (u16)cvtpk(ln, ln));
    } else if (w == 4) {
        lds_st<unsigned>(L_Z + l * 4, 0u);
        lds_st<unsigned>(L_ONE + l * 4, l < 32 ? 0x3f803f80u : 0xff800000u);
    }
    WAITV(1);
    KLD(0);
    WAITV(1);
    asm volatile("s_waitcnt lgkmcnt(0)\n\ts_barrier" : "+s"(ev) :: "memory");
    KLD(1);
    ST(1);
    k.tag = ev + 1u;
    k.tagv = k.tag;
    PIN(k.tagv);
    {
        if (w < 4) {
            const v4f zz = {0.f, 0.f, 0.f, 0.f};
            v4u bw[4], al[4];
#pragma unroll
            for (int ks = 0; ks < 4; ++ks) { bw[ks] = frag_tr(qa + ks * 4096, qa + ks * 4096 + 512); al[ks] = lds_ld<v4u>(la + ks * 64); }
            __builtin_amdgcn_sched_barrier(0);
            v4f acc = mfma(al[0], bw[0], zz);
#pragma unroll
            for (int ks = 1; ks < 4; ++ks) acc = mfma(al[ks], bw[ks], acc);
            put8(k.s_ws, qoff, __float_as_uint(acc.x), k.tagv);
        } else {
            asm volatile("buffer_store_dword %0, %0, %1, 0 offen" ::"v"(qoff), "s"(k.s_ws) : "memory");
        }
    }
    ST(2);

    unsigned dmy = 0;
    {
        unsigned qo = WS_Q + (unsigned)(gA * 2 + bA) * 16384u + (unsigned)(l >> 5) * 8192u + (unsigned)w * 512u + (unsigned)(l & 31) * 16u;
        PIN(qo);
        WAITV(2);
        KLD(2);
        WAITV(2);
        KLD(3);
        {
            const u16* p0 = prj ? wo + gP * 262144 : w1;
            const u16* p1 = prj ? wo + gP * 262144 + 262142 : w1 + 917502;
            const u16* p2 = prj ? out : w2 + fb * 16384;
            const u16* p = (l & 2) ? p2 : ((l & 1) ? p1 : p0);
            asm volatile("global_load_dword %0, %1, off" : "=&{v190}"(dmy) : "v"(p) : "memory");
        }
        unsigned qw = L_QI + (unsigned)(w >> 1) * 320u + (unsigned)(w & 1) * 128u + (unsigned)(l & 31) * 4u + (unsigned)(l >> 5) * 2u;
        unsigned aq = L_QI + (unsigned)(c >> 2) * 320u + (unsigned)gq * 16u;
        unsigned mxa = L_MX + (unsigned)gq * 16u;
        unsigned mxw = mxa + (unsigned)(c & 3) * 4u;
        unsigned pr = L_P + (unsigned)gq * 64u + (unsigned)(c & 3) * 16u;
        unsigned pw = L_P + (unsigned)(w >> 1) * 256u + (unsigned)(c >> 2) * 64u + (unsigned)gq * 16u + (unsigned)((w & 1) * 4 + (c & 3)) * 2u;
        const unsigned vrow = (unsigned)gq * 4u + k.q4;
        unsigned va = L_V + vrow * 256u + k.p4 * 8u + (((unsigned)w ^ (vrow & 7u)) * 32u);
        unsigned oo = l < 16 ? WS_O + (unsigned)(bA * 8 + gA) * 32768u + (unsigned)w * 4096u + (unsigned)s * 256u + (unsigned)l * 16u : OOB;
        PIN(qw); PIN(aq); PIN(mxa); PIN(mxw); PIN(pr); PIN(pw); PIN(va); PIN(oo);
        unsigned vvo1 = vvo ^ 128u;
        PIN(vvo); PIN(vvo1);
        WAITV(1 + TLBW);
        if (QD) __builtin_amdgcn_s_sleep(QD);
        ST(3);
        v4u g0, g1;
        const unsigned k4 = 4096u;
        asm volatile("buffer_load_dwordx4 %0, %2, %3, 0 offen sc1\n\t"
                     "buffer_load_dwordx4 %1, %2, %3, %4 offen sc1"
                     : "=&{v[160:163]}"(g0), "=&{v[164:167]}"(g1) : "v"(qo), "s"(k.s_ws), "s"(k4) : "memory");
        VLD(0);
        WAITV(3);
        VLD(1);
        v4u kf[4];
#pragma unroll
        for (int st = 0; st < 4; ++st) kf[st] = lds_ld<v4u>(L_K + (unsigned)(w * 16 + c) * 256u + (unsigned)(((st * 4 + gq) ^ c) * 16));
        {
            unsigned t, t2, cnt;
#define NTS " nt"
            asm volatile("s_movk_i32 %[cnt], " SPINS "\n\t"
                         "s_mov_b32 m0, %[m2]\n\t"
                         "s_waitcnt vmcnt(2)\n\t"
                         "v_bitop3_b32 %[t], v161, v163, %[tag] bitop3:0x7e\n\t"
                         "v_bitop3_b32 %[t2], v165, v167, %[tag] bitop3:0x7e\n\t"
                         "v_or_b32 %[t], %[t], %[t2]\n\t"
                         "v_cmp_ne_u32 vcc, 0, %[t]\n\t"
                         "s_cbranch_vccz 2f\n\t"
                         "buffer_load_dwordx4 v[160:163], %[o1], %[srd], 0 offen sc1\n\t"
                         "buffer_load_dwordx4 v[164:167], %[o1], %[srd], %[k4] offen sc1\n\t"
                         "s_sub_u32 %[cnt], %[cnt], 1\n"
                         "2:\n\t"
                         "buffer_load_dwordx4 %[v2], %[sv], %[kvb] offen offset:2048" NTS " lds\n\t"
                         "buffer_load_dwordx4 %[v3], %[sv], %[kvb] offen offset:3072" NTS " lds\n\t"
                         "s_cbranch_vccz 3f\n\t"
                         "s_waitcnt vmcnt(2)\n"
                         "1:\n\t"
                         "v_bitop3_b32 %[t], v161, v163, %[tag] bitop3:0x7e\n\t"
                         "v_bitop3_b32 %[t2], v165, v167, %[tag] bitop3:0x7e\n\t"
                         "v_or_b32 %[t], %[t], %[t2]\n\t"
                         "v_cmp_ne_u32 vcc, 0, %[t]\n\t"
                         "s_cbranch_vccz 3f\n\t"
                         "buffer_load_dwordx4 v[160:163], %[o1], %[srd], 0 offen sc1\n\t"
                         "buffer_load_dwordx4 v[164:167], %[o1], %[srd], %[k4] offen sc1\n\t"
                         "s_sub_u32 %[cnt], %[cnt], 1\n\t"
                         "s_waitcnt vmcnt(0)\n\t"
                         "s_cbranch_scc0 1b\n"
                         "3:"
                         : [t] "=&v"(t), [t2] "=&v"(t2), [cnt] "=&s"(cnt), "+{v[160:163]}"(g0), "+{v[164:167]}"(g1)
                         : [o1] "v"(qo), [srd] "s"(k.s_ws), [k4] "s"(k4), [tag] "s"(k.tag), [m2] "s"(L_V + w * 4096), [v2] "v"(vvo), [v3] "v"(vvo1),
                           [sv] "s"(s_v), [kvb] "s"(kvb)
                         : "memory", "vcc", "scc");
            const float qv = mix32(__uint_as_float(g0.x) + __uint_as_float(g1.x), __uint_as_float(g0.z) + __uint_as_float(g1.z));
            lds_st<u16>(qw, (u16)cvtpk(qv, qv));
        }
        BAR();
        ST(4);
        float s0;
        {
            v4u qf[4];
#pragma unroll
            for (int st = 0; st < 4; ++st) qf[st] = lds_ld<v4u>(aq + st * 64);
            __builtin_amdgcn_sched_barrier(0);
            v4f sc = {0.f, 0.f, 0.f, 0.f};
#pragma unroll
            for (int st = 0; st < 4; ++st) sc = mfma(qf[st], kf[st], sc);
            s0 = rb(rb(sc.x) * 0.08838834764831845f);
            __builtin_amdgcn_ds_fmaxf((LAS float*)(size_t)mxw, s0, 0, 0, false);
        }
        BAR();
        {
            const v4f m4 = lds_ld<v4f>(mxa);
            const float mx = vmax3(m4.x, m4.y, vmax(m4.z, m4.w));
            const float e0 = __builtin_amdgcn_exp2f((s0 - mx) * 1.4426950408889634f);
            lds_st<u16>(pw, (u16)cvtpk(e0, e0));
        }
        BAR();
        v4u pf[4];
#pragma unroll
        for (int j = 0; j < 4; ++j) pf[j] = lds_ld<v4u>(pr + j * 256);
        if (w == 7) {
            const v4f m4 = lds_ld<v4f>(L_MX + (unsigned)(c & 3) * 16u);
            const float mx = vmax3(m4.x, m4.y, vmax(m4.z, m4.w));
            const v4u ones = {0x3f803f80u, 0x3f803f80u, 0x3f803f80u, 0x3f803f80u};
            v4f ls = {0.f, 0.f, 0.f, 0.f};
#pragma unroll
            for (int j = 0; j < 4; ++j) ls = mfma(ones, pf[j], ls);
            put16(k.s_ws, l < 8 ? WS_ML + (unsigned)((bA * 8 + gA) * 16 + s) * 128u + (unsigned)l * 16u : OOB, __float_as_uint(mx), __float_as_uint(ls.x), k.tagv);
        }
        ST(12);
        if (prj) {
            prj_load<0, REP>(k, gP, cb, wo);
            asm volatile("s_cmp_eq_u32 %0, 7\n\ts_cbranch_scc1 1f\n\ts_waitcnt vmcnt(%1)\n1:\n\ts_waitcnt vmcnt(%2)" :: "s"(w), "n"(REP), "n"(REP + 1) : "memory", "scc");
        } else {
            mlp_load<0, REM>(k, fb, w1, w2);
            asm volatile("s_cmp_eq_u32 %0, 7\n\ts_cbranch_scc1 1f\n\ts_waitcnt vmcnt(%1)\n1:\n\ts_waitcnt vmcnt(%2)" :: "s"(w), "n"(REM), "n"(REM + 1) : "memory", "scc");
        }
        asm volatile("s_barrier" ::: "memory");
        ST(5);
        {
            v4u vf[4];
#pragma unroll
            for (int j = 0; j < 4; ++j) vf[j] = frag_tr(va + j * 8192, va + j * 8192 + 4096);
            __builtin_amdgcn_sched_barrier(0);
            v4f o = {0.f, 0.f, 0.f, 0.f};
#pragma unroll
            for (int j = 0; j < 4; ++j) o = mfma(pf[j], vf[j], o);
            put16(k.s_ws, oo, cvtpk(o.x, o.y), cvtpk(o.z, o.w), k.tagv);
        }
        __builtin_amdgcn_s_barrier();
        ST(6);
    }
    if (!prj) {
        if (s < 14) {
            if (MD) __builtin_amdgcn_s_sleep(MD);
            mlp_load<REM, 8>(k, fb, w1, w2);
            Mlp m;
            mlp_prep(STPASS k, L_R, bA, m);
            mlp_run(STPASS k, bA, fb, m);
        }
    } else {
        prj_load<REP, 8>(k, gP, cb, wo);
        unsigned oo = WS_O + (unsigned)(bA * 8 + gP) * 32768u + (unsigned)w * 4096u + (unsigned)l * 16u;
        unsigned mo = WS_ML + (unsigned)((bA * 8 + gP) * 16 + (c >> 2) * 4 + gq) * 128u + (unsigned)(c & 3) * 16u;
        unsigned ow = L_OS + (unsigned)w * 128u + (unsigned)l * 2u;
        unsigned ar[4];
#pragma unroll
        for (int t = 0; t < 4; ++t) { ar[t] = (c & 3) == t ? L_OS + (unsigned)w * 128u + (unsigned)gq * 16u : L_Z; PIN(ar[t]); }
        unsigned hw = L_HR + (unsigned)w * 256u + (unsigned)c * 16u;
        unsigned hr = L_HR + (unsigned)gq * 512u + (unsigned)c * 16u;
        unsigned hoff = (gq & 1) ? OOB : WS_H + (unsigned)((bA * 8 + cb) * 8 + gP) * 256u + (unsigned)(gq >> 1) * 128u + (unsigned)c * 8u;
        PIN(oo); PIN(mo); PIN(ow); PIN(hw); PIN(hr); PIN(hoff);
        v4u ml, o0, o1, o2, o3;
        if (PW < 63) { WAITV(PW); if (w != 4) ST(7); }
        {
            unsigned t, cnt;
            asm volatile("s_movk_i32 %[cnt], " SPINS "\n"
                         "1:\n\t"
                         "buffer_load_dwordx4 v[176:179], %[mo], %[srd], 0 offen sc1\n\t"
                         "buffer_load_dwordx4 v[160:163], %[off], %[srd], 0 offen sc1\n\t"
                         "buffer_load_dwordx4 v[164:167], %[off], %[srd], 0 offen offset:1024 sc1\n\t"
                         "buffer_load_dwordx4 v[168:171], %[off], %[srd], 0 offen offset:2048 sc1\n\t"
                         "buffer_load_dwordx4 v[172:175], %[off], %[srd], 0 offen offset:3072 sc1\n\t"
                         "s_sub_u32 %[cnt], %[cnt], 1\n\t"
                         "s_waitcnt vmcnt(4)\n\t"
                         "s_cbranch_scc1 2f\n\t"
                         "v_bitop3_b32 %[t], v177, v179, %[tag] bitop3:0x7e\n\t"
                         "v_cmp_ne_u32 vcc, 0, %[t]\n\t"
                         "s_cbranch_vccnz 1b\n"
                         "2:"
                         : [t] "=&v"(t), [cnt] "=&s"(cnt), "=&{v[160:163]}"(o0), "=&{v[164:167]}"(o1), "=&{v[168:171]}"(o2), "=&{v[172:175]}"(o3), "=&{v[176:179]}"(ml)
                         : [off] "v"(oo), [mo] "v"(mo), [srd] "s"(k.s_ws), [tag] "s"(k.tag) : "memory", "vcc", "scc");
        }
        ST(13);
        unsigned okv;
        {
            unsigned t2, t3;
            asm volatile("s_waitcnt vmcnt(0)\n\t"
                         "v_bitop3_b32 %[t], v161, v163, %[tag] bitop3:0x7e\n\t"
                         "v_bitop3_b32 %[t2], v165, v167, %[tag] bitop3:0x7e\n\t"
                         "v_bitop3_b32 %[t3], v169, v171, %[tag] bitop3:0x7e\n\t"
                         "v_or3_b32 %[t], %[t], %[t2], %[t3]\n\t"
                         "v_bitop3_b32 %[t2], v173, v175, %[tag] bitop3:0x7e\n\t"
                         "v_or_b32 %[t], %[t], %[t2]\n\t"
                         "v_cmp_ne_u32 vcc, 0, %[t]\n\t"
                         "s_cbranch_vccz 2f\n\t"
                         "buffer_load_dwordx4 v[160:163], %[off], %[srd], 0 offen sc1\n\t"
                         "buffer_load_dwordx4 v[164:167], %[off], %[srd], 0 offen offset:1024 sc1\n\t"
                         "buffer_load_dwordx4 v[168:171], %[off], %[srd], 0 offen offset:2048 sc1\n\t"
                         "buffer_load_dwordx4 v[172:175], %[off], %[srd], 0 offen offset:3072 sc1\n"
                         "2:"
                         : [t] "=&v"(okv), [t2] "=&v"(t2), [t3] "=&v"(t3), "+{v[160:163]}"(o0), "+{v[164:167]}"(o1), "+{v[168:171]}"(o2), "+{v[172:175]}"(o3)
                         : [off] "v"(oo), [srd] "s"(k.s_ws), [tag] "s"(k.tag) : "memory", "vcc");
        }
        v4u wf[4][2];
        {
            const unsigned a = L_R + (unsigned)w * 8192u + k.tr128;
#pragma unroll
            for (int t = 0; t < 4; ++t)
#pragma unroll
                for (int ks = 0; ks < 2; ++ks) { const unsigned q = a + (((unsigned)t ^ k.trsw) * 32u) + ks * 4096; wf[t][ks] = frag_tr(q, q + 512); }
        }
        float wt;
        {
            const float m = __uint_as_float(ml.x);
            float M = vmax(m, dppf<XOR8>(m));
            M = x32_max(x16_max(vmax(M, dppf<0x124>(M))));
            const float f = __builtin_amdgcn_exp2f((m - M) * 1.4426950408889634f);
            float L = f * __uint_as_float(ml.z);
            L += dppf<XOR8>(L);
            L += dppf<0x124>(L);
            L = x32_sum(x16_sum(L));
            wt = f * __builtin_amdgcn_rcpf(L);
        }
#define WB(n) dppf<0x150 + (n)>(wt)
        v2f W0 = {WB(0), WB(1)}, W1 = {WB(2), WB(3)}, W2 = {WB(4), WB(5)}, W3 = {WB(6), WB(7)};
        v2f W4 = {WB(8), WB(9)}, W5 = {WB(10), WB(11)}, W6 = {WB(12), WB(13)}, W7 = {WB(14), WB(15)};
        PIN(W0); PIN(W1); PIN(W2); PIN(W3); PIN(W4); PIN(W5); PIN(W6); PIN(W7);
#pragma unroll
        for (int t = 0; t < 4; ++t) { PIN(wf[t][0]); PIN(wf[t][1]); }
        {
            unsigned t, t2, t3, cnt;
            asm volatile("s_movk_i32 %[cnt], " SPINS "\n"
                         "1:\n\t"
                         "s_waitcnt vmcnt(2)\n\t"
                         "v_bitop3_b32 %[t], v161, v163, %[tag] bitop3:0x7e\n\t"
                         "v_bitop3_b32 %[t2], v165, v167, %[tag] bitop3:0x7e\n\t"
                         "s_waitcnt vmcnt(0)\n\t"
                         "v_bitop3_b32 %[t3], v169, v171, %[tag] bitop3:0x7e\n\t"
                         "v_or3_b32 %[t], %[t], %[t2], %[t3]\n\t"
                         "v_bitop3_b32 %[t2], v173, v175, %[tag] bitop3:0x7e\n\t"
                         "v_or_b32 %[t], %[t], %[t2]\n\t"
                         "v_cmp_ne_u32 vcc, 0, %[t]\n\t"
                         "s_cbranch_vccz 2f\n\t"
                         "s_sub_u32 %[cnt], %[cnt], 1\n\t"
                         "s_cbranch_scc1 2f\n\t"
                         "buffer_load_dwordx4 v[160:163], %[off], %[srd], 0 offen sc1\n\t"
                         "buffer_load_dwordx4 v[164:167], %[off], %[srd], 0 offen offset:1024 sc1\n\t"
                         "buffer_load_dwordx4 v[168:171], %[off], %[srd], 0 offen offset:2048 sc1\n\t"
                         "buffer_load_dwordx4 v[172:175], %[off], %[srd], 0 offen offset:3072 sc1\n\t"
                         "s_branch 1b\n"
                         "2:"
                         : [t] "=&v"(t), [t2] "=&v"(t2), [t3] "=&v"(t3), [cnt] "=&s"(cnt), "+{v[160:163]}"(o0), "+{v[164:167]}"(o1), "+{v[168:171]}"(o2), "+{v[172:175]}"(o3)
                         : [off] "v"(oo), [srd] "s"(k.s_ws), [tag] "s"(k.tag) : "memory", "vcc", "scc");
            ST(15);
#define UP(v) (v2f){bf_lo(v), bf_hi(v)}
            v2f a01 = UP(o0.x) * W0, a23 = UP(o0.z) * W1;
            a01 = __builtin_elementwise_fma(UP(o1.x), W2, a01); a23 = __builtin_elementwise_fma(UP(o1.z), W3, a23);
            a01 = __builtin_elementwise_fma(UP(o2.x), W4, a01); a23 = __builtin_elementwise_fma(UP(o2.z), W5, a23);
            a01 = __builtin_elementwise_fma(UP(o3.x), W6, a01); a23 = __builtin_elementwise_fma(UP(o3.z), W7, a23);
            const float v0 = a01.x, v1 = a01.y, v2 = a23.x, v3 = a23.y;
            const float oc = mix16(mix32(v0, v2), mix32(v1, v3));
            lds_st<u16>(ow, (u16)cvtpk(oc, oc));
            v4u af[4][2];
#pragma unroll
            for (int t = 0; t < 4; ++t) { af[t][0] = lds_ld<v4u>(ar[t]); af[t][1] = lds_ld<v4u>(ar[t] + 64); }
            __builtin_amdgcn_sched_barrier(0);
            v4f acc = {0.f, 0.f, 0.f, 0.f};
#pragma unroll
            for (int t = 0; t < 4; ++t) { acc = mfma(af[t][0], wf[t][0], acc); acc = mfma(af[t][1], wf[t][1], acc); }
            lds_st<v4f>(hw, acc);
        }
        BAR();
        if (w == 0) {
            const v4f t = lds_ld<v4f>(hr) + lds_ld<v4f>(hr + 256);
            const float a = x16_sum(mix32(t.x, t.z)), bq = x16_sum(mix32(t.y, t.w));
            put8(k.s_ws, hoff, cvtpk(a, bq), k.tagv);
            ST(14);
        }
        if (w == 4 && (u & 1) == 0) fin_run(STPASS k, xc >> 1, (xc & 1) * 4 + (u >> 3), (u >> 1) & 3, out);
    }
    asm volatile("" ::"{v190}"(dmy));
}

extern "C" void kernel_launch(void* const* d_in, const int* in_sizes, int n_in, void* d_out, int out_size, void* d_ws, size_t ws_size,
                              hipStream_t stream) {
    fused_block<<<256, 512, 0, stream>>>((const u16*)d_in[0], (const u16*)d_in[1], (const u16*)d_in[2], (const u16*)d_in[3], (const u16*)d_in[4],
                                         (const u16*)d_in[6], (const u16*)d_in[7], (u16*)d_out, (unsigned char*)d_ws);
}
```

```cpp
#include <hip/hip_runtime.h>
#include <hip/hip_bf16.h>

typedef unsigned short u16;
typedef unsigned long long u64;
typedef unsigned v4u __attribute__((ext_vector_type(4)));
typedef unsigned v2u __attribute__((ext_vector_type(2)));
typedef int v4i __attribute__((ext_vector_type(4)));
typedef float v4f __attribute__((ext_vector_type(4)));
typedef float v2f __attribute__((ext_vector_type(2)));
typedef short v4s __attribute__((ext_vector_type(4)));
typedef __bf16 v8bf __attribute__((ext_vector_type(8)));
typedef __bf16 v2bf __attribute__((ext_vector_type(2)));
#define LAS __attribute__((address_space(3)))
#define DEV __device__ __forceinline__

#define REP 2
#define REM 1
#define MD 0
#define PW 63
#define TLBW 1
#define NTA 1
#define NTW 1

constexpr unsigned WS_Q = 4096;
constexpr unsigned WS_ML = WS_Q + 262144;
constexpr unsigned WS_O = WS_ML + 32768;
constexpr unsigned WS_H = WS_O + 262144;
constexpr unsigned WS_Y = WS_H + 32768;
constexpr unsigned WS_END = WS_Y + 229376;

constexpr unsigned L_K = 0;
constexpr unsigned L_V = 32768;
constexpr unsigned L_R = 65536;
constexpr unsigned L_C = 131072;
constexpr unsigned L_LN = 147456;
constexpr unsigned L_QI = L_LN + 640;
constexpr unsigned L_Z = L_QI + 1280;
constexpr unsigned L_MX = L_Z + 192;
constexpr unsigned L_P = L_MX + 64;
constexpr unsigned L_OS = L_P + 1024;
constexpr unsigned L_HR = L_OS + 1024;
constexpr unsigned L_ZR = L_HR + 2048;
constexpr unsigned L_GS = L_ZR + 2560;
constexpr unsigned L_ZE = L_GS + 128;
constexpr unsigned L_X = L_ZE + 1024;
constexpr unsigned L_END = L_X + 2048;

DEV float bf_lo(unsigned v) { return __uint_as_float(v << 16); }
DEV float bf_hi(unsigned v) { return __uint_as_float(v & 0xffff0000u); }
DEV unsigned cvtpk(float a, float b) { v2f t = {a, b}; return __builtin_bit_cast(unsigned, __builtin_convertvector(t, v2bf)); }
DEV float rb(float a) { return bf_lo(cvtpk(a, a)); }
DEV float vmax(float a, float b) { return __builtin_elementwise_maximum(a, b); }
DEV float vmax3(float a, float b, float c) { return vmax(a, vmax(b, c)); }
template <int CTRL> DEV float dppf(float x) { return __builtin_bit_cast(float, __builtin_amdgcn_mov_dpp(__builtin_bit_cast(int, x), CTRL, 0xf, 0xf, true)); }
template <int CTRL> DEV unsigned dppu(unsigned x) { return (unsigned)__builtin_amdgcn_mov_dpp((int)x, CTRL, 0xf, 0xf, true); }
constexpr int XOR1 = 0xB1, XOR2 = 0x4E, XOR7 = 0x141, XOR8 = 0x128;
DEV float mix16(float a, float b) { auto s = __builtin_amdgcn_permlane16_swap(__float_as_uint(a), __float_as_uint(b), false, false); return __uint_as_float(s[0]) + __uint_as_float(s[1]); }
DEV float mix32(float a, float b) { auto s = __builtin_amdgcn_permlane32_swap(__float_as_uint(a), __float_as_uint(b), false, false); return __uint_as_float(s[0]) + __uint_as_float(s[1]); }
DEV float x16_sum(float x) { return mix16(x, x); }
DEV float x32_sum(float x) { return mix32(x, x); }
DEV float x16_max(float x) { auto s = __builtin_amdgcn_permlane16_swap(__float_as_uint(x), __float_as_uint(x), false, false); return vmax(__uint_as_float(s[0]), __uint_as_float(s[1])); }
DEV float x32_max(float x) { auto s = __builtin_amdgcn_permlane32_swap(__float_as_uint(x), __float_as_uint(x), false, false); return vmax(__uint_as_float(s[0]), __uint_as_float(s[1])); }
DEV float sum8(float x) { x += dppf<XOR1>(x); x += dppf<XOR2>(x); x += dppf<XOR7>(x); return x; }
DEV float sum16(float x) { x = sum8(x); x += dppf<XOR8>(x); return x; }
DEV float dot2(unsigned a, unsigned b, float c) { return __builtin_amdgcn_fdot2_f32_bf16(__builtin_bit_cast(v2bf, a), __builtin_bit_cast(v2bf, b), c, false); }
DEV v4f mfma(v4u a, v4u b, v4f c) { return __builtin_amdgcn_mfma_f32_16x16x32_bf16(__builtin_bit_cast(v8bf, a), __builtin_bit_cast(v8bf, b), c, 0, 0, 0); }
DEV v4i mksrd(const void* p, unsigned n) { u64 a = (u64)p; v4i r; r.x = (int)(unsigned)a; r.y = (int)(unsigned)(a >> 32); r.z = (int)n; r.w = 0x00020000; return r; }

template <typename T> DEV T lds_ld(unsigned a) { return *(LAS T*)(size_t)a; }
template <typename T> DEV void lds_st(unsigned a, T v) { *(LAS T*)(size_t)a = v; }
DEV v2u lds_tr(unsigned a) { return __builtin_bit_cast(v2u, __builtin_amdgcn_ds_read_tr16_b64_v4i16((LAS v4s*)(size_t)a)); }
DEV v4u frag_tr(unsigned a0, unsigned a1) { v2u p = lds_tr(a0), q = lds_tr(a1); v4u r = {p.x, p.y, q.x, q.y}; return r; }
#define PIN(x) asm volatile("" : "+v"(x))

template <int NT> DEV void dma(unsigned ldsdst, unsigned voff, v4i srd, unsigned soff) {
    if (NT) asm volatile("s_mov_b32 m0, %0\n\ts_nop 0\n\tbuffer_load_dwordx4 %1, %2, %3 offen nt lds" ::"s"(ldsdst), "v"(voff), "s"(srd), "s"(soff) : "memory");
    else asm volatile("s_mov_b32 m0, %0\n\ts_nop 0\n\tbuffer_load_dwordx4 %1, %2, %3 offen lds" ::"s"(ldsdst), "v"(voff), "s"(srd), "s"(soff) : "memory");
}
#define WAITV(n) asm volatile("s_waitcnt vmcnt(%0)" ::"n"(n) : "memory")
#define BAR() asm volatile("s_waitcnt lgkmcnt(0)\n\ts_barrier" ::: "memory")
constexpr unsigned OOB = 0x7ff00000u;
DEV void put16(v4i srd, unsigned voff, unsigned a, unsigned b, unsigned tagv) {
    v4u v = {a, tagv, b, tagv};
    asm volatile("buffer_store_dwordx4 %0, %1, %2, 0 offen sc1\n\ts_nop 1" ::"v"(v), "v"(voff), "s"(srd) : "memory");
}
DEV void put8(v4i srd, unsigned voff, unsigned a, unsigned tagv) {
    v2u v = {a, tagv};
    asm volatile("buffer_store_dwordx2 %0, %1, %2, 0 offen sc1\n\ts_nop 1" ::"v"(v), "v"(voff), "s"(srd) : "memory");
}

#define SPINS "0x3000"
#define ST(i) do { } while (0)
#define STARG
#define STPASS

struct Ctx {
    int l, w, gq, c;
    unsigned q4, p4, p8, trsw, tr128;
    unsigned tag, tagv;
    v4i s_ws, s_x;
};

template <int I0, int I1> DEV void mlp_load(const Ctx& k, int fb, const u16* w1, const u16* w2) {
    const v4i s_w1 = mksrd(w1, 1835008u), s_w2 = mksrd(w2, 1835008u);
    const unsigned vo = (unsigned)(k.l >> 2) * 3584u + (unsigned)(((k.l & 3) ^ ((k.l >> 5) << 1)) * 16);
    const unsigned so = (unsigned)(k.w * 64) * 3584u + (unsigned)fb * 64u;
    const unsigned vo2 = (unsigned)(k.l >> 3) * 1024u + k.p8;
    const unsigned so2 = (unsigned)(fb * 32) * 1024u + (unsigned)k.w * 128u;
#pragma unroll
    for (int i = I0; i < I1; ++i) {
        if (i < 4) dma<NTW>(L_R + k.w * 8192 + i * 1024, vo, s_w1, so + i * 16 * 3584);
        else dma<NTW>(L_R + k.w * 8192 + i * 1024, vo2 ^ (unsigned)((i & 1) * 64), s_w2, so2 + (i - 4) * 8192);
    }
}
template <int I0, int I1> DEV void prj_load(const Ctx& k, int gP, int dh, int cb, const u16* wo) {
    const v4i s_wo = mksrd(wo, 4u << 20);
    const unsigned vo = (unsigned)(k.l >> 3) * 1024u + k.p8;
    const unsigned so = (unsigned)(gP * 512 + (k.w >> 2) * 256 + (dh * 4 + (k.w & 3)) * 16) * 1024u + (unsigned)cb * 128u;
    const unsigned lo = L_R + (unsigned)((k.w & 3) * 8192 + (k.w >> 2) * 4096);
#pragma unroll
    for (int i = I0; i < I1; ++i) dma<NTW>(lo + i * 1024, vo ^ (unsigned)((i & 1) * 64), s_wo, so + ((i >> 1) * 128 + (i & 1) * 8) * 1024);
}
DEV void mlp_run(STARG const Ctx& k, int bM, int fb, const u16* w2) {
    const int l = k.l, w = k.w, gq = k.gq, c = k.c;
    const v4f zz = {0.f, 0.f, 0.f, 0.f};
    v4u ones = {0x3f803f80u, 0x3f803f80u, 0x3f803f80u, 0x3f803f80u};
    mlp_load<4, 8>(k, fb, w2, w2);
    WAITV(4);
    ST(13);
    v4u f1[2][2], f2[4];
    float cs;
    {
        const unsigned a = L_R + (unsigned)w * 8192u + ((k.q4 & 1) * 16 + (unsigned)gq * 4 + (k.q4 >> 1)) * 64u + k.p4 * 8u;
        const unsigned sx = (unsigned)(gq >> 1) * 32u;
#pragma unroll
        for (int ks = 0; ks < 2; ++ks) {
            f1[ks][0] = frag_tr(a + sx + ks * 2048, a + sx + ks * 2048 + 128);
            f1[ks][1] = frag_tr(a + (sx ^ 32u) + ks * 2048, a + (sx ^ 32u) + ks * 2048 + 128);
        }
        const v4f d0 = mfma(ones, f1[1][0], mfma(ones, f1[0][0], zz)), d1 = mfma(ones, f1[1][1], mfma(ones, f1[0][1], zz));
        lds_st<float>(L_ZE + (unsigned)w * 128u + (unsigned)(l & 31) * 4u, (gq & 1) ? d1.x : d0.x);
        BAR();
        const unsigned r = L_ZE + (unsigned)(c >> 3) * 512u + (unsigned)(gq * 8 + (c & 7)) * 4u;
        cs = (lds_ld<float>(r) + lds_ld<float>(r + 128)) + (lds_ld<float>(r + 256) + lds_ld<float>(r + 384));
        cs += dppf<XOR8>(cs);
    }
    const unsigned hs = L_C + (unsigned)w * 2048u;
    unsigned ho = WS_H + (unsigned)(bM * 8 + w) * 2048u + (unsigned)(l >> 3) * 128u + (unsigned)(((l & 7) ^ gq) * 16), ho1 = ho ^ 64u;
    unsigned ha = hs + (unsigned)c * 128u + (unsigned)((gq ^ (c >> 1)) * 16), ha1 = ha ^ 64u;
    unsigned za = L_ZR + (unsigned)((gq >> 1) * 16 + c) * 80u + (unsigned)(w * 2 + (gq & 1)) * 4u;
    unsigned ga = L_GS + (unsigned)(w * 16 + gq * 4);
    unsigned zr = L_ZR + (unsigned)(gq * 8 + (c & 7)) * 80u + (unsigned)(c >> 3) * 32u;
    unsigned gr = L_GS + (unsigned)(l & 7) * 16u;
    unsigned yo = (gq & 1) ? OOB : WS_Y + ((unsigned)(bM * 16 + w * 2 + (gq >> 1)) * 56u + (unsigned)fb) * 128u + (unsigned)c * 8u;
    PIN(ho); PIN(ho1); PIN(ha); PIN(ha1); PIN(za); PIN(ga); PIN(zr); PIN(gr); PIN(yo); PIN(cs); PIN(ones);
    WAITV(0);
    ST(14);
    {
        const unsigned a2 = L_R + (unsigned)w * 8192u + 4096u + k.tr128;
#pragma unroll
        for (int nt = 0; nt < 4; ++nt) { const unsigned q = a2 + (((unsigned)nt ^ k.trsw) * 32u); f2[nt] = frag_tr(q, q + 512); }
    }
    ST(7);
    v4u x0, x1;
    {
        unsigned t, t2, cnt;
        asm volatile("s_movk_i32 %[cnt], " SPINS "\n\t"
                     "s_mov_b32 m0, %[hs]\n"
                     "1:\n\t"
                     "buffer_load_dwordx4 %[o0], %[srd], 0 offen sc1 lds\n\t"
                     "buffer_load_dwordx4 %[o1], %[srd], 0 offen offset:1024 sc1 lds\n\t"
                     "s_sub_u32 %[cnt], %[cnt], 1\n\t"
                     "s_waitcnt vmcnt(0)\n\t"
                     "ds_read_b128 v[160:163], %[a0]\n\t"
                     "ds_read_b128 v[164:167], %[a1]\n\t"
                     "s_waitcnt lgkmcnt(0)\n\t"
                     "s_cbranch_scc1 2f\n\t"
                     "v_max3_u32 %[t], v160, v161, v162\n\t"
                     "v_max3_u32 %[t2], v163, v164, v165\n\t"
                     "v_max3_u32 %[t], %[t], v166, v167\n\t"
                     "v_max_u32 %[t], %[t], %[t2]\n\t"
                     "v_cmp_eq_u32 vcc, -1, %[t]\n\t"
                     "s_cbranch_vccnz 1b\n"
                     "2:"
                     : [t] "=&v"(t), [t2] "=&v"(t2), [cnt] "=&s"(cnt), "=&{v[160:163]}"(x0), "=&{v[164:167]}"(x1)
                     : [o0] "v"(ho), [o1] "v"(ho1), [srd] "s"(k.s_ws), [hs] "s"(hs), [a0] "v"(ha), [a1] "v"(ha1) : "memory", "vcc", "scc");
    }
    ST(8);
    {
        v4f gm = mfma(x0, x0, zz), rs = mfma(ones, x0, zz);
        gm = mfma(x1, x1, gm); rs = mfma(ones, x1, rs);
        v4f d0 = mfma(x0, f1[0][0], zz), d1 = mfma(x0, f1[0][1], zz);
        d0 = mfma(x1, f1[1][0], d0); d1 = mfma(x1, f1[1][1], d1);
        lds_st<float>(ga, sum16(mix32((gm.x + gm.y) + (gm.z + gm.w), rs.x)));
        lds_st<float>(za, mix32((d0.x + d0.y) + (d0.z + d0.w), (d1.x + d1.y) + (d1.z + d1.w)));
    }
    BAR();
    {
        const v4f g4 = lds_ld<v4f>(gr);
        const v4f za4 = lds_ld<v4f>(zr), zb4 = lds_ld<v4f>(zr + 16);
        const float s2 = sum8(g4.x + g4.y) * (1.0f / 512.0f), mean = sum8(g4.z) * (1.0f / 1024.0f);
        const v4f zs = za4 + zb4;
        float z = (zs.x + zs.y) + (zs.z + zs.w);
        z += dppf<XOR8>(z);
        const float var = vmax(s2 - mean * mean, 0.f);
        const float mr = rb(mean), den = rb(__builtin_amdgcn_sqrtf(rb(rb(var) + 1e-5f)));
        z = rb((z - mr * cs) * __builtin_amdgcn_rcpf(den));
        const float hv = z * __builtin_amdgcn_rcpf(1.0f + __builtin_amdgcn_exp2f(z * -1.4426950408889634f));
        const unsigned pk = cvtpk(hv, dppf<0x101>(hv));
        const v4u af = {pk, dppu<0x102>(pk), dppu<0x104>(pk), dppu<0x106>(pk)};
        const v4f y0 = mfma(af, f2[0], zz), y2 = mfma(af, f2[2], zz), y1 = mfma(af, f2[1], zz), y3 = mfma(af, f2[3], zz);
        const bool hi = gq >= 2;
        put8(k.s_ws, yo, __float_as_uint(hi ? y2.x : y0.x), __float_as_uint(hi ? y3.x : y1.x));
    }
    ST(9);
}
DEV void fin_run(STARG const Ctx& k, int bF, int cF, int s, u16* out) {
    const int l = k.l, n7 = l & 7;
    const int col = cF * 64 + (s >> 1) * 32 + (s & 1) * 8 + n7;
    unsigned h0, h1;
    {
        const unsigned ho = WS_H + (unsigned)((bF * 8 + cF) * 16 + (l >> 3)) * 128u + (unsigned)(s * 8 + n7) * 4u;
        unsigned t, cnt;
        asm volatile("s_movk_i32 %[cnt], " SPINS "\n"
                     "1:\n\t"
                     "buffer_load_dword %[h0], %[o], %[srd], 0 offen sc1\n\t"
                     "buffer_load_dword %[h1], %[o], %[srd], 0 offen offset:1024 sc1\n\t"
                     "s_sub_u32 %[cnt], %[cnt], 1\n\t"
                     "s_waitcnt vmcnt(0)\n\t"
                     "s_cbranch_scc1 2f\n\t"
                     "v_max_u32 %[t], %[h0], %[h1]\n\t"
                     "v_cmp_eq_u32 vcc, -1, %[t]\n\t"
                     "s_cbranch_vccnz 1b\n"
                     "2:"
                     : [t] "=&v"(t), [cnt] "=&s"(cnt), [h0] "=&v"(h0), [h1] "=&v"(h1) : [o] "v"(ho), [srd] "s"(k.s_ws) : "memory", "vcc", "scc");
    }
    float a = mix32(bf_lo(h0) + bf_lo(h1), bf_hi(h0) + bf_hi(h1));
    a += dppf<XOR8>(a);
    float hh = rb(x16_sum(a));
    unsigned yo = WS_Y + (unsigned)(bF * 16 + cF * 2 + (s >> 1)) * 7168u + (unsigned)(l >> 3) * 128u + (unsigned)((s & 1) * 64 + n7 * 8), yo2 = yo + 4096u;
    const v4i s_out = mksrd(out, 2048);
    unsigned oo = (l & 24) ? OOB : (unsigned)(bF * 512 + col) * 2u + (unsigned)(l >> 5) * 32u;
    PIN(hh); PIN(yo); PIN(yo2); PIN(oo);
    ST(7);
    v2f y0, y1, y2, y3, y4, y5, y6;
    unsigned t, cnt;
    asm volatile("s_movk_i32 %[cnt], " SPINS "\n"
                 "1:\n\t"
                 "buffer_load_dwordx2 v[160:161], %[o1], %[srd], 0 offen sc1\n\t"
                 "buffer_load_dwordx2 v[162:163], %[o1], %[srd], 0 offen offset:1024 sc1\n\t"
                 "buffer_load_dwordx2 v[164:165], %[o1], %[srd], 0 offen offset:2048 sc1\n\t"
                 "buffer_load_dwordx2 v[166:167], %[o1], %[srd], 0 offen offset:3072 sc1\n\t"
                 "buffer_load_dwordx2 v[168:169], %[o2], %[srd], 0 offen sc1\n\t"
                 "buffer_load_dwordx2 v[170:171], %[o2], %[srd], 0 offen offset:1024 sc1\n\t"
                 "buffer_load_dwordx2 v[172:173], %[o2], %[srd], 0 offen offset:2048 sc1\n\t"
                 "s_sub_u32 %[cnt], %[cnt], 1\n\t"
                 "s_waitcnt vmcnt(4)\n\t"
                 "v_max3_u32 %[t], v161, v163, v165\n\t"
                 "s_waitcnt vmcnt(2)\n\t"
                 "v_max3_u32 %[t], %[t], v167, v169\n\t"
                 "s_waitcnt vmcnt(0)\n\t"
                 "s_cbranch_scc1 2f\n\t"
                 "v_max3_u32 %[t], %[t], v171, v173\n\t"
                 "v_cmp_eq_u32 vcc, -1, %[t]\n\t"
                 "s_cbranch_vccnz 1b\n"
                 "2:"
                 : [t] "=&v"(t), [cnt] "=&s"(cnt), "=&{v[160:161]}"(y0), "=&{v[162:163]}"(y1), "=&{v[164:165]}"(y2),
                   "=&{v[166:167]}"(y3), "=&{v[168:169]}"(y4), "=&{v[170:171]}"(y5), "=&{v[172:173]}"(y6)
                 : [o1] "v"(yo), [o2] "v"(yo2), [srd] "s"(k.s_ws) : "memory", "vcc", "scc");
    ST(8);
    const v2f ys = ((y0 + y1) + (y2 + y3)) + ((y4 + y5) + y6);
    float y = mix32(ys.x, ys.y);
    y += dppf<XOR8>(y);
    y = x16_sum(y);
    const unsigned r = cvtpk(hh + rb(y), 0.f);
    asm volatile("buffer_store_short %0, %1, %2, 0 offen" ::"v"(r), "v"(oo), "s"(s_out) : "memory");
    if ((bF | cF | s) == 0) {
        const unsigned zo = l ? OOB : 0u;
        asm volatile("buffer_store_dword %0, %1, %2, 0 offen sc1" ::"v"(k.tagv), "v"(zo), "s"(k.s_ws) : "memory");
    }
    ST(9);
}

__global__ void __launch_bounds__(512, 2)
fused_block(const u16* __restrict__ x, const u16* __restrict__ kc, const u16* __restrict__ vc, const u16* __restrict__ wq,
            const u16* __restrict__ wo, const u16* __restrict__ w1, const u16* __restrict__ w2, u16* __restrict__ out,
            unsigned char* __restrict__ ws) {
    __shared__ __attribute__((aligned(1024))) unsigned char lds_raw[L_END];
    asm volatile("" ::"v"((LAS unsigned char*)lds_raw));
    const int tid = threadIdx.x, l = tid & 63;
    const int w = __builtin_amdgcn_readfirstlane(tid >> 6);
    const int xc = blockIdx.x & 7, u = blockIdx.x >> 3;
    const int gq = l >> 4, c = l & 15;
    const int kq = u & 3, cq = u >> 2, gQ = (xc + 3) & 7;
    const int bA = u >> 4, gA = (xc + 7) & 7, s = u & 15;
    const bool prj = xc < 4, mlp = s < 14;
    const int gP = xc * 2 + ((u >> 3) & 1), cb = u & 7, dh = u >> 4;
    const int fb = (xc - 4) * 14 + (s < 14 ? s : 13);
    Ctx k;
    k.l = l; k.w = w; k.gq = gq; k.c = c;
    k.s_ws = mksrd(ws, WS_END); k.s_x = mksrd(x, 2048);
    unsigned ev;
    asm volatile("s_load_dword %0, %1, 0x0" : "=s"(ev) : "s"(ws), "s"(kc), "s"(vc), "s"(wq), "s"(wo), "s"(w1), "s"(w2), "s"(out) : "memory");

    unsigned xv; v4u xa;
    {
        const unsigned xo = (unsigned)((w >> 1) & 1) * 1024u + (unsigned)l * 16u, xvo = (unsigned)(((tid >> 7) & 1) * 512 + kq * 128 + (tid & 127)) * 2u;
        asm volatile("buffer_load_dwordx4 %0, %2, %3, 0 offen\n\t"
                     "buffer_load_ushort %1, %4, %3, 0 offen"
                     : "=&{v[160:163]}"(xa), "=&{v164}"(xv) : "v"(xo), "s"(k.s_x), "v"(xvo) : "memory");
    }
    k.p8 = (unsigned)(((l & 7) ^ (((l >> 4) & 1) << 1)) * 16);
    {
        const v4i s_wq = mksrd(wq, 4u << 20);
        const unsigned vo = (unsigned)(l >> 3) * 8192u + k.p8;
        const unsigned so = (unsigned)(kq * 128 + w * 16) * 8192u + (unsigned)(gQ * 512 + cq * 64) * 2u;
        dma<NTA>(L_C + w * 2048, vo, s_wq, so);
        dma<NTA>(L_C + w * 2048 + 1024, vo ^ 64u, s_wq, so + 8u * 8192u);
    }
    {
        const unsigned sb = w < 4 ? (u == 0 ? WS_H + (unsigned)w * 8192u + (unsigned)gQ * 256u : OOB)
                          : w == 4 ? WS_O + (unsigned)(bA * 8 + gQ) * 16384u + (unsigned)s * 1024u
                          : w == 5 ? WS_Y + blockIdx.x * 896u : OOB;
        unsigned co = sb + (w < 4 ? (unsigned)gq * 2048u + (unsigned)c * 16u : (unsigned)l * 16u);
        if (w == 5 && l >= 56) co = OOB;
        const v4u m1 = {~0u, ~0u, ~0u, ~0u};
        asm volatile("buffer_store_dwordx4 %0, %1, %2, 0 offen sc1\n\ts_nop 1" ::"v"(m1), "v"(co), "s"(k.s_ws) : "memory");
    }
    const v4i s_k = mksrd(kc, 32u << 20), s_v = mksrd(vc, 32u << 20);
    const unsigned kvb = (unsigned)((bA * 8 + gA) * 8192 + 6144 + s * 128 + w * 16) * 256u;
    unsigned vvo = (unsigned)gq * 256u + (unsigned)((c ^ (gq << 1)) * 16);
    unsigned kvo = (unsigned)gq * 256u + (unsigned)((c ^ gq) * 16);
#define KLD(i) dma<NTA>(L_K + (w * 4 + (i)) * 1024, kvo ^ (unsigned)((i) * 64), s_k, kvb + (i) * 1024)
#define VLD(i) dma<NTA>(L_V + (w * 4 + (i)) * 1024, vvo ^ (unsigned)(((i) & 1) * 128), s_v, kvb + (i) * 1024)
    k.q4 = (unsigned)((l >> 2) & 3); k.p4 = (unsigned)(l & 3);
    k.trsw = (k.q4 >> 1) | ((unsigned)(gq & 1) << 1);
    k.tr128 = (unsigned)(gq * 8 + k.q4) * 128u + k.p4 * 8u;
    unsigned qa = L_C + k.tr128 + (((unsigned)w ^ k.trsw) * 32u);
    unsigned la = L_LN + (unsigned)((c >> 2) & 1) * 320u + (unsigned)gq * 16u;
    unsigned qoff = (l < 32 && w < 4) ? WS_Q + (unsigned)((((gQ * 2 + gq) * 4 + kq) * 512) + cq * 64 + w * 16 + c) * 8u : OOB;
    PIN(qa); PIN(la); PIN(qoff);
    asm volatile("s_waitcnt vmcnt(%2)" : "+{v[160:163]}"(xa), "+{v164}"(xv) : "n"(3) : "memory");
    if (w < 4) {
        const float s1 = dot2(xa.w, 0x3f803f80u, dot2(xa.z, 0x3f803f80u, dot2(xa.y, 0x3f803f80u, dot2(xa.x, 0x3f803f80u, 0.f))));
        const float s2 = dot2(xa.w, xa.w, dot2(xa.z, xa.z, dot2(xa.y, xa.y, dot2(xa.x, xa.x, 0.f))));
        const float t = x16_sum(sum16(mix32(s1, s2))) * (1.0f / 512.0f);
        const float mean = __builtin_bit_cast(float, __builtin_amdgcn_readlane(__builtin_bit_cast(int, t), 0));
        const float msq = __builtin_bit_cast(float, __builtin_amdgcn_readlane(__builtin_bit_cast(int, t), 32));
        const float var = vmax(msq - mean * mean, 0.f);
        const float mr = rb(mean), den = rb(__builtin_amdgcn_sqrtf(rb(rb(var) + 1e-5f)));
        const float ln = __fdiv_rn(rb(bf_lo(xv) - mr), den);
        lds_st<u16>(L_LN + (tid >> 7) * 320 + (tid & 127) * 2, (u16)cvtpk(ln, ln));
    } else if (w == 4) {
        lds_st<unsigned>(L_Z + l * 4, l < 48 ? 0u : 0xff800000u);
    } else if (w < 7) {
        lds_st<v4u>(L_X + (unsigned)(w - 5) * 1024u + (unsigned)l * 16u, xa);
    }
    WAITV(2);
    KLD(0);
    WAITV(1);
    asm volatile("s_waitcnt lgkmcnt(0)\n\ts_barrier" : "+s"(ev) :: "memory");
    KLD(1);
    ST(1);
    k.tag = ev + 1u;
    k.tagv = k.tag;
    PIN(k.tagv);
    {
        if (w < 4) {
            const v4f zz = {0.f, 0.f, 0.f, 0.f};
            v4u bw[4], al[4];
#pragma unroll
            for (int ks = 0; ks < 4; ++ks) { bw[ks] = frag_tr(qa + ks * 4096, qa + ks * 4096 + 512); al[ks] = lds_ld<v4u>(la + ks * 64); }
            __builtin_amdgcn_sched_barrier(0);
            v4f acc = mfma(al[0], bw[0], zz);
#pragma unroll
            for (int ks = 1; ks < 4; ++ks) acc = mfma(al[ks], bw[ks], acc);
            put8(k.s_ws, qoff, __float_as_uint(acc.x), k.tagv);
        } else {
            asm volatile("buffer_store_dword %0, %0, %1, 0 offen" ::"v"(qoff), "s"(k.s_ws) : "memory");
        }
    }
    ST(2);

    unsigned dmy = 0;
    {
        unsigned qo = WS_Q + (unsigned)(gA * 2 + bA) * 16384u + (unsigned)(l >> 5) * 8192u + (unsigned)w * 512u + (unsigned)(l & 31) * 16u;
        PIN(qo);
        WAITV(2);
        KLD(2);
        WAITV(2);
        KLD(3);
        {
            const u16* p0 = prj ? wo + gP * 262144 : w1;
            const u16* p1 = prj ? wo + gP * 262144 + 262142 : w1 + 917502;
            const u16* p2 = prj ? out : w2 + fb * 16384;
            const u16* p = (l & 2) ? p2 : ((l & 1) ? p1 : p0);
            asm volatile("global_load_dword %0, %1, off" : "=&{v190}"(dmy) : "v"(p) : "memory");
        }
        unsigned qw = L_QI + (unsigned)(w >> 1) * 320u + (unsigned)(w & 1) * 128u + (unsigned)(l & 31) * 4u + (unsigned)(l >> 5) * 2u;
        unsigned aq = L_QI + (unsigned)(c >> 2) * 320u + (unsigned)gq * 16u;
        unsigned mxa = L_MX + (unsigned)gq * 16u;
        unsigned mxw = mxa + (unsigned)(c & 3) * 4u;
        unsigned pr = L_P + (unsigned)gq * 64u + (unsigned)(c & 3) * 16u;
        unsigned pw = L_P + (unsigned)(w >> 1) * 256u + (unsigned)(c >> 2) * 64u + (unsigned)gq * 16u + (unsigned)((w & 1) * 4 + (c & 3)) * 2u;
        const unsigned vrow = (unsigned)gq * 4u + k.q4;
        unsigned va = L_V + vrow * 256u + k.p4 * 8u + (((unsigned)w ^ (vrow & 7u)) * 32u);
        unsigned oo = l < 16 ? WS_O + (unsigned)(bA * 8 + gA) * 16384u + (unsigned)w * 2048u + (unsigned)s * 128u + (unsigned)l * 8u : OOB;
        unsigned ck = WS_Q + 4u + ((prj && l < 32) ? (unsigned)gP * 32768u + (unsigned)(l & 3) * 4096u + (unsigned)(l >> 2) * 512u : (unsigned)(l & 7) * 32768u);
        PIN(qw); PIN(aq); PIN(mxa); PIN(mxw); PIN(pr); PIN(pw); PIN(va); PIN(oo); PIN(ck);
        unsigned vvo1 = vvo ^ 128u;
        PIN(vvo); PIN(vvo1);
        WAITV(1 + TLBW);
        ST(3);
        v4u g0, g1;
        const unsigned k4 = 4096u;
        asm volatile("buffer_load_dwordx4 %0, %2, %3, 0 offen sc1\n\t"
                     "buffer_load_dwordx4 %1, %2, %3, %4 offen sc1"
                     : "=&{v[160:163]}"(g0), "=&{v[164:167]}"(g1) : "v"(qo), "s"(k.s_ws), "s"(k4) : "memory");
        VLD(0);
        WAITV(3 + TLBW);
        VLD(1);
        v4u kf[4];
#pragma unroll
        for (int st = 0; st < 4; ++st) kf[st] = lds_ld<v4u>(L_K + (unsigned)(w * 16 + c) * 256u + (unsigned)(((st * 4 + gq) ^ c) * 16));
        {
            unsigned t, t2, cnt;
#define NTS " nt"
            asm volatile("s_movk_i32 %[cnt], " SPINS "\n\t"
                         "s_mov_b32 m0, %[m2]\n\t"
                         "s_waitcnt vmcnt(2)\n\t"
                         "v_bitop3_b32 %[t], v161, v163, %[tag] bitop3:0x7e\n\t"
                         "v_bitop3_b32 %[t2], v165, v167, %[tag] bitop3:0x7e\n\t"
                         "v_or_b32 %[t], %[t], %[t2]\n\t"
                         "v_cmp_ne_u32 vcc, 0, %[t]\n\t"
                         "s_cbranch_vccz 2f\n\t"
                         "buffer_load_dwordx4 v[160:163], %[o1], %[srd], 0 offen sc1\n\t"
                         "buffer_load_dwordx4 v[164:167], %[o1], %[srd], %[k4] offen sc1\n\t"
                         "s_sub_u32 %[cnt], %[cnt], 1\n"
                         "2:\n\t"
                         "buffer_load_dwordx4 %[v2], %[sv], %[kvb] offen offset:2048" NTS " lds\n\t"
                         "buffer_load_dwordx4 %[v3], %[sv], %[kvb] offen offset:3072" NTS " lds\n\t"
                         "s_cbranch_vccz 3f\n\t"
                         "s_waitcnt vmcnt(2)\n"
                         "1:\n\t"
                         "v_bitop3_b32 %[t], v161, v163, %[tag] bitop3:0x7e\n\t"
                         "v_bitop3_b32 %[t2], v165, v167, %[tag] bitop3:0x7e\n\t"
                         "v_or_b32 %[t], %[t], %[t2]\n\t"
                         "v_cmp_ne_u32 vcc, 0, %[t]\n\t"
                         "s_cbranch_vccz 3f\n\t"
                         "buffer_load_dwordx4 v[160:163], %[o1], %[srd], 0 offen sc1\n\t"
                         "buffer_load_dwordx4 v[164:167], %[o1], %[srd], %[k4] offen sc1\n\t"
                         "s_sub_u32 %[cnt], %[cnt], 1\n\t"
                         "s_waitcnt vmcnt(0)\n\t"
                         "s_cbranch_scc0 1b\n"
                         "3:"
                         : [t] "=&v"(t), [t2] "=&v"(t2), [cnt] "=&s"(cnt), "+{v[160:163]}"(g0), "+{v[164:167]}"(g1)
                         : [o1] "v"(qo), [srd] "s"(k.s_ws), [k4] "s"(k4), [tag] "s"(k.tag), [m2] "s"(L_V + w * 4096), [v2] "v"(vvo), [v3] "v"(vvo1),
                           [sv] "s"(s_v), [kvb] "s"(kvb)
                         : "memory", "vcc", "scc");
            const float qv = mix32(__uint_as_float(g0.x) + __uint_as_float(g1.x), __uint_as_float(g0.z) + __uint_as_float(g1.z));
            lds_st<u16>(qw, (u16)cvtpk(qv, qv));
        }
        BAR();
        ST(4);
        float s0;
        {
            v4u qf[4];
#pragma unroll
            for (int st = 0; st < 4; ++st) qf[st] = lds_ld<v4u>(aq + st * 64);
            __builtin_amdgcn_sched_barrier(0);
            v4f sc = {0.f, 0.f, 0.f, 0.f};
#pragma unroll
            for (int st = 0; st < 4; ++st) sc = mfma(qf[st], kf[st], sc);
            s0 = rb(rb(sc.x) * 0.08838834764831845f);
            __builtin_amdgcn_ds_fmaxf((LAS float*)(size_t)mxw, s0, 0, 0, false);
        }
        BAR();
        {
            const v4f m4 = lds_ld<v4f>(mxa);
            const float mx = vmax3(m4.x, m4.y, vmax(m4.z, m4.w));
            const float e0 = __builtin_amdgcn_exp2f((s0 - mx) * 1.4426950408889634f);
            lds_st<u16>(pw, (u16)cvtpk(e0, e0));
        }
        BAR();
        v4u pf[4];
#pragma unroll
        for (int j = 0; j < 4; ++j) pf[j] = lds_ld<v4u>(pr + j * 256);
        unsigned ct;
        asm volatile("s_cmp_lg_u32 %1, 6\n\ts_cbranch_scc1 1f\n\tbuffer_load_dword %0, %2, %3, 0 offen sc1\n1:"
                     : "=&{v168}"(ct) : "s"(w), "v"(ck), "s"(k.s_ws) : "memory", "scc");
        if (w == 7) {
            const v4f m4 = lds_ld<v4f>(L_MX + (unsigned)(c & 3) * 16u);
            const float mx = vmax3(m4.x, m4.y, vmax(m4.z, m4.w));
            const v4u ones = {0x3f803f80u, 0x3f803f80u, 0x3f803f80u, 0x3f803f80u};
            v4f ls = {0.f, 0.f, 0.f, 0.f};
#pragma unroll
            for (int j = 0; j < 4; ++j) ls = mfma(ones, pf[j], ls);
            put16(k.s_ws, l < 8 ? WS_ML + (unsigned)((bA * 8 + gA) * 16 + s) * 128u + (unsigned)l * 16u : OOB, __float_as_uint(mx), __float_as_uint(ls.x), k.tagv);
        }
        ST(12);
        if (prj) {
            prj_load<0, REP>(k, gP, dh, cb, wo);
            asm volatile("s_cmp_gt_u32 %0, 5\n\ts_cbranch_scc1 1f\n\ts_waitcnt vmcnt(%1)\n1:\n\ts_waitcnt vmcnt(%2)" :: "s"(w), "n"(REP), "n"(REP + 1) : "memory", "scc");
        } else {
            mlp_load<0, REM>(k, fb, w1, w2);
            asm volatile("s_cmp_gt_u32 %0, 5\n\ts_cbranch_scc1 1f\n\ts_waitcnt vmcnt(%1)\n1:\n\ts_waitcnt vmcnt(%2)" :: "s"(w), "n"(REM), "n"(REM + 1) : "memory", "scc");
        }
        asm volatile("s_barrier" ::: "memory");
        ST(5);
        {
            v4u vf[4];
#pragma unroll
            for (int j = 0; j < 4; ++j) vf[j] = frag_tr(va + j * 8192, va + j * 8192 + 4096);
            __builtin_amdgcn_sched_barrier(0);
            v4f o = {0.f, 0.f, 0.f, 0.f};
#pragma unroll
            for (int j = 0; j < 4; ++j) o = mfma(pf[j], vf[j], o);
            put8(k.s_ws, oo, cvtpk(o.x, o.y), cvtpk(o.z, o.w));
        }
        {
            unsigned cnt;
            asm volatile("s_cmp_lg_u32 %[w], 6\n\t"
                         "s_cbranch_scc1 3f\n\t"
                         "s_movk_i32 %[cnt], " SPINS "\n\t"
                         "s_cmp_lt_u32 %[pj], 4\n\t"
                         "s_cbranch_scc1 4f\n\t"
                         "s_waitcnt vmcnt(%[nm])\n\t"
                         "s_branch 1f\n"
                         "4:\n\t"
                         "s_waitcnt vmcnt(%[np])\n"
                         "1:\n\t"
                         "v_cmp_ne_u32 vcc, %[tag], v168\n\t"
                         "s_cbranch_vccz 3f\n\t"
                         "buffer_load_dword v168, %[ck], %[srd], 0 offen sc1\n\t"
                         "s_sub_u32 %[cnt], %[cnt], 1\n\t"
                         "s_waitcnt vmcnt(0)\n\t"
                         "s_cbranch_scc0 1b\n"
                         "3:\n\t"
                         "s_barrier"
                         : [cnt] "=&s"(cnt), "+{v168}"(ct)
                         : [w] "s"(w), [pj] "s"(xc), [nm] "n"(REM + 1), [np] "n"(REP + 1), [tag] "s"(k.tag), [ck] "v"(ck), [srd] "s"(k.s_ws)
                         : "memory", "vcc", "scc");
        }
        ST(6);
    }
    if (!prj) {
        if (mlp) {
            if (MD) __builtin_amdgcn_s_sleep(MD);
            mlp_load<REM, 4>(k, fb, w1, w2);
            mlp_run(STPASS k, bA, fb, w2);
        }
    } else {
        prj_load<REP, 4>(k, gP, dh, cb, wo);
        const int bP = w >> 2;
        unsigned oo = WS_O + (unsigned)(bP * 8 + gP) * 16384u + (unsigned)(dh * 4 + (w & 3)) * 2048u + (unsigned)l * 8u;
        unsigned mo = WS_ML + (unsigned)((bP * 8 + gP) * 16 + (c >> 2) * 4 + gq) * 128u + (unsigned)(c & 3) * 16u;
        PIN(oo); PIN(mo);
        v4u ml;
        v2u o0, o1, o2, o3;
        if (PW < 63) WAITV(PW);
#define OLDS "buffer_load_dwordx2 v[160:161], %[off], %[srd], 0 offen sc1\n\t"             \
             "buffer_load_dwordx2 v[162:163], %[off], %[srd], 0 offen offset:512 sc1\n\t"  \
             "buffer_load_dwordx2 v[164:165], %[off], %[srd], 0 offen offset:1024 sc1\n\t" \
             "buffer_load_dwordx2 v[166:167], %[off], %[srd], 0 offen offset:1536 sc1\n\t"
        asm volatile("buffer_load_dwordx4 v[176:179], %[mo], %[srd], 0 offen sc1\n\t" OLDS
                     : "=&{v[160:161]}"(o0), "=&{v[162:163]}"(o1), "=&{v[164:165]}"(o2), "=&{v[166:167]}"(o3), "=&{v[176:179]}"(ml)
                     : [off] "v"(oo), [mo] "v"(mo), [srd] "s"(k.s_ws) : "memory");
        unsigned ow = L_OS + (unsigned)w * 128u + (unsigned)l * 2u;
        unsigned ar[4];
#pragma unroll
        for (int t = 0; t < 4; ++t) { ar[t] = (c & 3) == t ? L_OS + (unsigned)w * 128u + (unsigned)gq * 16u : L_Z; PIN(ar[t]); }
        unsigned hw = L_HR + (unsigned)w * 256u + (unsigned)c * 16u;
        unsigned hr = hw + (unsigned)gq * 256u;
        unsigned hoff = (gq & 1) ? OOB : WS_H + (unsigned)((bP * 8 + cb) * 16 + gP * 2 + dh) * 128u + (unsigned)(gq >> 1) * 64u + (unsigned)c * 4u;
        v4f acc;
        {
            const unsigned xr = (((gP | dh | (w & 3)) == 0) ? L_X + (unsigned)bP * 1024u + (unsigned)cb * 128u : L_Z) + (unsigned)c * 2u;
            acc.x = bf_lo(lds_ld<u16>(xr)); acc.y = bf_lo(lds_ld<u16>(xr + 32)); acc.z = bf_lo(lds_ld<u16>(xr + 64)); acc.w = bf_lo(lds_ld<u16>(xr + 96));
        }
        PIN(ow); PIN(hw); PIN(hr); PIN(hoff); PIN(acc);
        asm volatile("s_waitcnt vmcnt(5)\n\ts_barrier" ::: "memory");
        if (w != 2) ST(7);
        v4u wf[4][2];
        {
            const unsigned a = L_R + (unsigned)(w & 3) * 8192u + k.tr128;
#pragma unroll
            for (int t = 0; t < 4; ++t)
#pragma unroll
                for (int ks = 0; ks < 2; ++ks) { const unsigned q = a + (((unsigned)t ^ k.trsw) * 32u) + ks * 4096; wf[t][ks] = frag_tr(q, q + 512); }
        }
        {
            unsigned t, cnt;
            asm volatile("s_movk_i32 %[cnt], " SPINS "\n"
                         "1:\n\t"
                         "s_waitcnt vmcnt(4)\n\t"
                         "v_bitop3_b32 %[t], v177, v179, %[tag] bitop3:0x7e\n\t"
                         "v_cmp_ne_u32 vcc, 0, %[t]\n\t"
                         "s_cbranch_vccz 2f\n\t"
                         "s_sub_u32 %[cnt], %[cnt], 1\n\t"
                         "s_cbranch_scc1 2f\n\t"
                         "buffer_load_dwordx4 v[176:179], %[mo], %[srd], 0 offen sc1\n\t" OLDS
                         "s_branch 1b\n"
                         "2:\n\t"
                         "s_waitcnt vmcnt(0)\n\t"
                         "v_max3_u32 %[t], v161, v163, v165\n\t"
                         "v_max_u32 %[t], %[t], v167\n\t"
                         "v_cmp_eq_u32 vcc, -1, %[t]\n\t"
                         "s_cbranch_vccz 3f\n\t" OLDS
                         "3:"
                         : [t] "=&v"(t), [cnt] "=&s"(cnt), "+{v[160:161]}"(o0), "+{v[162:163]}"(o1), "+{v[164:165]}"(o2), "+{v[166:167]}"(o3), "+{v[176:179]}"(ml)
                         : [off] "v"(oo), [mo] "v"(mo), [srd] "s"(k.s_ws), [tag] "s"(k.tag) : "memory", "vcc", "scc");
        }
        ST(13);
        float wt;
        {
            const float m = __uint_as_float(ml.x);
            float M = vmax(m, dppf<XOR8>(m));
            M = x32_max(x16_max(vmax(M, dppf<0x124>(M))));
            const float f = __builtin_amdgcn_exp2f((m - M) * 1.4426950408889634f);
            float L = f * __uint_as_float(ml.z);
            L += dppf<XOR8>(L);
            L += dppf<0x124>(L);
            L = x32_sum(x16_sum(L));
            wt = f * __builtin_amdgcn_rcpf(L);
        }
#define WB(n) dppf<0x150 + (n)>(wt)
        v2f W0 = {WB(0), WB(1)}, W1 = {WB(2), WB(3)}, W2 = {WB(4), WB(5)}, W3 = {WB(6), WB(7)};
        v2f W4 = {WB(8), WB(9)}, W5 = {WB(10), WB(11)}, W6 = {WB(12), WB(13)}, W7 = {WB(14), WB(15)};
        PIN(W0); PIN(W1); PIN(W2); PIN(W3); PIN(W4); PIN(W5); PIN(W6); PIN(W7);
#pragma unroll
        for (int t = 0; t < 4; ++t) { PIN(wf[t][0]); PIN(wf[t][1]); }
        {
            unsigned t, cnt;
            asm volatile("s_movk_i32 %[cnt], " SPINS "\n"
                         "1:\n\t"
                         "s_waitcnt vmcnt(0)\n\t"
                         "v_max3_u32 %[t], v161, v163, v165\n\t"
                         "v_max_u32 %[t], %[t], v167\n\t"
                         "v_cmp_eq_u32 vcc, -1, %[t]\n\t"
                         "s_cbranch_vccz 2f\n\t"
                         "s_sub_u32 %[cnt], %[cnt], 1\n\t"
                         "s_cbranch_scc1 2f\n\t" OLDS
                         "s_branch 1b\n"
                         "2:"
                         : [t] "=&v"(t), [cnt] "=&s"(cnt), "+{v[160:161]}"(o0), "+{v[162:163]}"(o1), "+{v[164:165]}"(o2), "+{v[166:167]}"(o3)
                         : [off] "v"(oo), [srd] "s"(k.s_ws) : "memory", "vcc", "scc");
            ST(15);
#define UP(v) (v2f){bf_lo(v), bf_hi(v)}
            v2f a01 = UP(o0.x) * W0, a23 = UP(o0.y) * W1;
            a01 = __builtin_elementwise_fma(UP(o1.x), W2, a01); a23 = __builtin_elementwise_fma(UP(o1.y), W3, a23);
            a01 = __builtin_elementwise_fma(UP(o2.x), W4, a01); a23 = __builtin_elementwise_fma(UP(o2.y), W5, a23);
            a01 = __builtin_elementwise_fma(UP(o3.x), W6, a01); a23 = __builtin_elementwise_fma(UP(o3.y), W7, a23);
            const float v0 = a01.x, v1 = a01.y, v2 = a23.x, v3 = a23.y;
            const float oc = mix16(mix32(v0, v2), mix32(v1, v3));
            lds_st<u16>(ow, (u16)cvtpk(oc, oc));
            v4u af[4][2];
#pragma unroll
            for (int t = 0; t < 4; ++t) { af[t][0] = lds_ld<v4u>(ar[t]); af[t][1] = lds_ld<v4u>(ar[t] + 64); }
            __builtin_amdgcn_sched_barrier(0);
#pragma unroll
            for (int t = 0; t < 4; ++t) { acc = mfma(af[t][0], wf[t][0], acc); acc = mfma(af[t][1], wf[t][1], acc); }
            lds_st<v4f>(hw, acc);
        }
        BAR();
        if ((w & 3) == 0) {
            const v4f t = lds_ld<v4f>(hr);
            const float a = x16_sum(mix32(t.x, t.z)), bq = x16_sum(mix32(t.y, t.w));
            const unsigned hv = cvtpk(a, bq);
            asm volatile("buffer_store_dword %0, %1, %2, 0 offen sc1" ::"v"(hv), "v"(hoff), "s"(k.s_ws) : "memory");
            ST(14);
        }
        if (w == 2 && (u & 1) == 0) fin_run(STPASS k, xc >> 1, (xc & 1) * 4 + (u >> 3), (u >> 1) & 3, out);
    }
    asm volatile("" ::"{v190}"(dmy));
}

extern "C" void kernel_launch(void* const* d_in, const int* in_sizes, int n_in, void* d_out, int out_size, void* d_ws, size_t ws_size,
                              hipStream_t stream) {
    fused_block<<<256, 512, 0, stream>>>((const u16*)d_in[0], (const u16*)d_in[1], (const u16*)d_in[2], (const u16*)d_in[3], (const u16*)d_in[4],
                                         (const u16*)d_in[6], (const u16*)d_in[7], (u16*)d_out, (unsigned char*)d_ws);
}
```

```cpp
#include <hip/hip_runtime.h>
#include <hip/hip_bf16.h>

typedef unsigned short u16;
typedef unsigned long long u64;
typedef unsigned v4u __attribute__((ext_vector_type(4)));
typedef unsigned v2u __attribute__((ext_vector_type(2)));
typedef int v4i __attribute__((ext_vector_type(4)));
typedef float v4f __attribute__((ext_vector_type(4)));
typedef float v2f __attribute__((ext_vector_type(2)));
typedef short v4s __attribute__((ext_vector_type(4)));
typedef __bf16 v8bf __attribute__((ext_vector_type(8)));
typedef __bf16 v2bf __attribute__((ext_vector_type(2)));
#define LAS __attribute__((address_space(3)))
#define DEV __device__ __forceinline__

#define REP 2
#define REM 1
#define MD 0
#define PW 0
#define NTA 1
#define NTW 1

constexpr unsigned WS_F = 1024;
constexpr unsigned WS_Q = 4096;
constexpr unsigned WS_ML = WS_Q + 262144;
constexpr unsigned WS_O = WS_ML + 32768;
constexpr unsigned WS_H = WS_O + 262144;
constexpr unsigned WS_Y = WS_H + 32768;
constexpr unsigned WS_END = WS_Y + 229376;

constexpr unsigned L_K = 0;
constexpr unsigned L_V = 32768;
constexpr unsigned L_R = 65536;
constexpr unsigned L_C = 131072;
constexpr unsigned L_LN = 147456;
constexpr unsigned L_QI = L_LN + 640;
constexpr unsigned L_Z = L_QI + 1280;
constexpr unsigned L_MX = L_Z + 192;
constexpr unsigned L_P = L_MX + 64;
constexpr unsigned L_OS = L_P + 1024;
constexpr unsigned L_HR = L_OS + 1024;
constexpr unsigned L_ZR = L_HR + 2048;
constexpr unsigned L_GS = L_ZR + 2560;
constexpr unsigned L_ZE = L_GS + 128;
constexpr unsigned L_X = L_ZE + 1024;
constexpr unsigned L_END = L_X + 2048;

DEV float bf_lo(unsigned v) { return __uint_as_float(v << 16); }
DEV float bf_hi(unsigned v) { return __uint_as_float(v & 0xffff0000u); }
DEV unsigned cvtpk(float a, float b) { v2f t = {a, b}; return __builtin_bit_cast(unsigned, __builtin_convertvector(t, v2bf)); }
DEV float rb(float a) { return bf_lo(cvtpk(a, a)); }
DEV float vmax(float a, float b) { return __builtin_elementwise_maximum(a, b); }
DEV float vmax3(float a, float b, float c) { return vmax(a, vmax(b, c)); }
template <int CTRL> DEV float dppf(float x) { return __builtin_bit_cast(float, __builtin_amdgcn_mov_dpp(__builtin_bit_cast(int, x), CTRL, 0xf, 0xf, true)); }
template <int CTRL> DEV unsigned dppu(unsigned x) { return (unsigned)__builtin_amdgcn_mov_dpp((int)x, CTRL, 0xf, 0xf, true); }
constexpr int XOR1 = 0xB1, XOR2 = 0x4E, XOR7 = 0x141, XOR8 = 0x128;
DEV float mix16(float a, float b) { auto s = __builtin_amdgcn_permlane16_swap(__float_as_uint(a), __float_as_uint(b), false, false); return __uint_as_float(s[0]) + __uint_as_float(s[1]); }
DEV float mix32(float a, float b) { auto s = __builtin_amdgcn_permlane32_swap(__float_as_uint(a), __float_as_uint(b), false, false); return __uint_as_float(s[0]) + __uint_as_float(s[1]); }
DEV float x16_sum(float x) { return mix16(x, x); }
DEV float x32_sum(float x) { return mix32(x, x); }
DEV float x16_max(float x) { auto s = __builtin_amdgcn_permlane16_swap(__float_as_uint(x), __float_as_uint(x), false, false); return vmax(__uint_as_float(s[0]), __uint_as_float(s[1])); }
DEV float x32_max(float x) { auto s = __builtin_amdgcn_permlane32_swap(__float_as_uint(x), __float_as_uint(x), false, false); return vmax(__uint_as_float(s[0]), __uint_as_float(s[1])); }
DEV float sum8(float x) { x += dppf<XOR1>(x); x += dppf<XOR2>(x); x += dppf<XOR7>(x); return x; }
DEV float sum16(float x) { x = sum8(x); x += dppf<XOR8>(x); return x; }
DEV float dot2(unsigned a, unsigned b, float c) { return __builtin_amdgcn_fdot2_f32_bf16(__builtin_bit_cast(v2bf, a), __builtin_bit_cast(v2bf, b), c, false); }
DEV v4f mfma(v4u a, v4u b, v4f c) { return __builtin_amdgcn_mfma_f32_16x16x32_bf16(__builtin_bit_cast(v8bf, a), __builtin_bit_cast(v8bf, b), c, 0, 0, 0); }
DEV v4i mksrd(const void* p, unsigned n) { u64 a = (u64)p; v4i r; r.x = (int)(unsigned)a; r.y = (int)(unsigned)(a >> 32); r.z = (int)n; r.w = 0x00020000; return r; }

template <typename T> DEV T lds_ld(unsigned a) { return *(LAS T*)(size_t)a; }
template <typename T> DEV void lds_st(unsigned a, T v) { *(LAS T*)(size_t)a = v; }
DEV v2u lds_tr(unsigned a) { return __builtin_bit_cast(v2u, __builtin_amdgcn_ds_read_tr16_b64_v4i16((LAS v4s*)(size_t)a)); }
DEV v4u frag_tr(unsigned a0, unsigned a1) { v2u p = lds_tr(a0), q = lds_tr(a1); v4u r = {p.x, p.y, q.x, q.y}; return r; }
#define PIN(x) asm volatile("" : "+v"(x))

template <int NT> DEV void dma(unsigned ldsdst, unsigned voff, v4i srd, unsigned soff) {
    if (NT) asm volatile("s_mov_b32 m0, %0\n\ts_nop 0\n\tbuffer_load_dwordx4 %1, %2, %3 offen nt lds" ::"s"(ldsdst), "v"(voff), "s"(srd), "s"(soff) : "memory");
    else asm volatile("s_mov_b32 m0, %0\n\ts_nop 0\n\tbuffer_load_dwordx4 %1, %2, %3 offen lds" ::"s"(ldsdst), "v"(voff), "s"(srd), "s"(soff) : "memory");
}
#define WAITV(n) asm volatile("s_waitcnt vmcnt(%0)" ::"n"(n) : "memory")
#define BAR() asm volatile("s_waitcnt lgkmcnt(0)\n\ts_barrier" ::: "memory")
constexpr unsigned OOB = 0x7ff00000u;
DEV void put16(v4i srd, unsigned voff, unsigned a, unsigned b, unsigned tagv) {
    v4u v = {a, tagv, b, tagv};
    asm volatile("buffer_store_dwordx4 %0, %1, %2, 0 offen sc1\n\ts_nop 1" ::"v"(v), "v"(voff), "s"(srd) : "memory");
}
DEV void put8(v4i srd, unsigned voff, unsigned a, unsigned tagv) {
    v2u v = {a, tagv};
    asm volatile("buffer_store_dwordx2 %0, %1, %2, 0 offen sc1\n\ts_nop 1" ::"v"(v), "v"(voff), "s"(srd) : "memory");
}

#define SPINS "0x3000"
#define ST(i) do { } while (0)
#define STARG
#define STPASS

struct Ctx {
    int l, w, gq, c;
    unsigned q4, p4, p8, trsw, tr128;
    unsigned tag, tagv;
    v4i s_ws, s_x;
};

template <int I0, int I1> DEV void mlp_load(const Ctx& k, int fb, const u16* w1, const u16* w2) {
    const v4i s_w1 = mksrd(w1, 1835008u), s_w2 = mksrd(w2, 1835008u);
    const unsigned vo = (unsigned)(k.l >> 2) * 3584u + (unsigned)(((k.l & 3) ^ ((k.l >> 5) << 1)) * 16);
    const unsigned so = (unsigned)(k.w * 64) * 3584u + (unsigned)fb * 64u;
    const unsigned vo2 = (unsigned)(k.l >> 3) * 1024u + k.p8;
    const unsigned so2 = (unsigned)(fb * 32) * 1024u + (unsigned)k.w * 128u;
#pragma unroll
    for (int i = I0; i < I1; ++i) {
        if (i < 4) dma<NTW>(L_R + k.w * 8192 + i * 1024, vo, s_w1, so + i * 16 * 3584);
        else dma<NTW>(L_R + k.w * 8192 + i * 1024, vo2 ^ (unsigned)((i & 1) * 64), s_w2, so2 + (i - 4) * 8192);
    }
}
template <int I0, int I1> DEV void prj_load(const Ctx& k, int gP, int dh, int cb, const u16* wo) {
    const v4i s_wo = mksrd(wo, 4u << 20);
    const unsigned vo = (unsigned)(k.l >> 3) * 1024u + k.p8;
    const unsigned so = (unsigned)(gP * 512 + (k.w >> 2) * 256 + (dh * 4 + (k.w & 3)) * 16) * 1024u + (unsigned)cb * 128u;
    const unsigned lo = L_R + (unsigned)((k.w & 3) * 8192 + (k.w >> 2) * 4096);
#pragma unroll
    for (int i = I0; i < I1; ++i) dma<NTW>(lo + i * 1024, vo ^ (unsigned)((i & 1) * 64), s_wo, so + ((i >> 1) * 128 + (i & 1) * 8) * 1024);
}
DEV void mlp_run(STARG const Ctx& k, int bM, int fb, const u16* w2) {
    const int l = k.l, w = k.w, gq = k.gq, c = k.c;
    const v4f zz = {0.f, 0.f, 0.f, 0.f};
    v4u ones = {0x3f803f80u, 0x3f803f80u, 0x3f803f80u, 0x3f803f80u};
    mlp_load<4, 8>(k, fb, w2, w2);
    WAITV(4);
    ST(13);
    v4u f1[2][2], f2[4];
    float cs;
    {
        const unsigned a = L_R + (unsigned)w * 8192u + ((k.q4 & 1) * 16 + (unsigned)gq * 4 + (k.q4 >> 1)) * 64u + k.p4 * 8u;
        const unsigned sx = (unsigned)(gq >> 1) * 32u;
#pragma unroll
        for (int ks = 0; ks < 2; ++ks) {
            f1[ks][0] = frag_tr(a + sx + ks * 2048, a + sx + ks * 2048 + 128);
            f1[ks][1] = frag_tr(a + (sx ^ 32u) + ks * 2048, a + (sx ^ 32u) + ks * 2048 + 128);
        }
        const v4f d0 = mfma(ones, f1[1][0], mfma(ones, f1[0][0], zz)), d1 = mfma(ones, f1[1][1], mfma(ones, f1[0][1], zz));
        lds_st<float>(L_ZE + (unsigned)w * 128u + (unsigned)(l & 31) * 4u, (gq & 1) ? d1.x : d0.x);
        BAR();
        const unsigned r = L_ZE + (unsigned)(c >> 3) * 512u + (unsigned)(gq * 8 + (c & 7)) * 4u;
        cs = (lds_ld<float>(r) + lds_ld<float>(r + 128)) + (lds_ld<float>(r + 256) + lds_ld<float>(r + 384));
        cs += dppf<XOR8>(cs);
    }
    const unsigned hs = L_C + (unsigned)w * 2048u;
    unsigned ho = WS_H + (unsigned)(bM * 8 + w) * 2048u + (unsigned)(l >> 3) * 128u + (unsigned)(((l & 7) ^ gq) * 16), ho1 = ho ^ 64u;
    unsigned ha = hs + (unsigned)c * 128u + (unsigned)((gq ^ (c >> 1)) * 16), ha1 = ha ^ 64u;
    unsigned za = L_ZR + (unsigned)((gq >> 1) * 16 + c) * 80u + (unsigned)(w * 2 + (gq & 1)) * 4u;
    unsigned ga = L_GS + (unsigned)(w * 16 + gq * 4);
    unsigned zr = L_ZR + (unsigned)(gq * 8 + (c & 7)) * 80u + (unsigned)(c >> 3) * 32u;
    unsigned gr = L_GS + (unsigned)(l & 7) * 16u;
    unsigned yo = (gq & 1) ? OOB : WS_Y + ((unsigned)(bM * 16 + w * 2 + (gq >> 1)) * 56u + (unsigned)fb) * 128u + (unsigned)c * 8u;
    PIN(ho); PIN(ho1); PIN(ha); PIN(ha1); PIN(za); PIN(ga); PIN(zr); PIN(gr); PIN(yo); PIN(cs); PIN(ones);
    WAITV(0);
    ST(14);
    {
        const unsigned a2 = L_R + (unsigned)w * 8192u + 4096u + k.tr128;
#pragma unroll
        for (int nt = 0; nt < 4; ++nt) { const unsigned q = a2 + (((unsigned)nt ^ k.trsw) * 32u); f2[nt] = frag_tr(q, q + 512); }
    }
    ST(7);
    v4u x0, x1;
    {
        unsigned t, t2, cnt;
        asm volatile("s_movk_i32 %[cnt], " SPINS "\n\t"
                     "s_mov_b32 m0, %[hs]\n"
                     "1:\n\t"
                     "buffer_load_dwordx4 %[o0], %[srd], 0 offen sc1 lds\n\t"
                     "buffer_load_dwordx4 %[o1], %[srd], 0 offen offset:1024 sc1 lds\n\t"
                     "s_sub_u32 %[cnt], %[cnt], 1\n\t"
                     "s_waitcnt vmcnt(0)\n\t"
                     "ds_read_b128 v[160:163], %[a0]\n\t"
                     "ds_read_b128 v[164:167], %[a1]\n\t"
                     "s_waitcnt lgkmcnt(0)\n\t"
                     "s_cbranch_scc1 2f\n\t"
                     "v_max3_u32 %[t], v160, v161, v162\n\t"
                     "v_max3_u32 %[t2], v163, v164, v165\n\t"
                     "v_max3_u32 %[t], %[t], v166, v167\n\t"
                     "v_max_u32 %[t], %[t], %[t2]\n\t"
                     "v_cmp_eq_u32 vcc, -1, %[t]\n\t"
                     "s_cbranch_vccnz 1b\n"
                     "2:"
                     : [t] "=&v"(t), [t2] "=&v"(t2), [cnt] "=&s"(cnt), "=&{v[160:163]}"(x0), "=&{v[164:167]}"(x1)
                     : [o0] "v"(ho), [o1] "v"(ho1), [srd] "s"(k.s_ws), [hs] "s"(hs), [a0] "v"(ha), [a1] "v"(ha1) : "memory", "vcc", "scc");
    }
    ST(8);
    {
        v4f gm = mfma(x0, x0, zz), rs = mfma(ones, x0, zz);
        gm = mfma(x1, x1, gm); rs = mfma(ones, x1, rs);
        v4f d0 = mfma(x0, f1[0][0], zz), d1 = mfma(x0, f1[0][1], zz);
        d0 = mfma(x1, f1[1][0], d0); d1 = mfma(x1, f1[1][1], d1);
        lds_st<float>(ga, sum16(mix32((gm.x + gm.y) + (gm.z + gm.w), rs.x)));
        lds_st<float>(za, mix32((d0.x + d0.y) + (d0.z + d0.w), (d1.x + d1.y) + (d1.z + d1.w)));
    }
    BAR();
    {
        const v4f g4 = lds_ld<v4f>(gr);
        const v4f za4 = lds_ld<v4f>(zr), zb4 = lds_ld<v4f>(zr + 16);
        const float s2 = sum8(g4.x + g4.y) * (1.0f / 512.0f), mean = sum8(g4.z) * (1.0f / 1024.0f);
        const v4f zs = za4 + zb4;
        float z = (zs.x + zs.y) + (zs.z + zs.w);
        z += dppf<XOR8>(z);
        const float var = vmax(s2 - mean * mean, 0.f);
        const float mr = rb(mean), den = rb(__builtin_amdgcn_sqrtf(rb(rb(var) + 1e-5f)));
        z = rb((z - mr * cs) * __builtin_amdgcn_rcpf(den));
        const float hv = z * __builtin_amdgcn_rcpf(1.0f + __builtin_amdgcn_exp2f(z * -1.4426950408889634f));
        const unsigned pk = cvtpk(hv, dppf<0x101>(hv));
        const v4u af = {pk, dppu<0x102>(pk), dppu<0x104>(pk), dppu<0x106>(pk)};
        const v4f y0 = mfma(af, f2[0], zz), y2 = mfma(af, f2[2], zz), y1 = mfma(af, f2[1], zz), y3 = mfma(af, f2[3], zz);
        const bool hi = gq >= 2;
        put8(k.s_ws, yo, __float_as_uint(hi ? y2.x : y0.x), __float_as_uint(hi ? y3.x : y1.x));
    }
    ST(9);
}
DEV void fin_run(STARG const Ctx& k, int bF, int cF, int s, u16* out) {
    const int l = k.l, n7 = l & 7;
    const int col = cF * 64 + (s >> 1) * 32 + (s & 1) * 8 + n7;
    unsigned h0, h1;
    {
        const unsigned ho = WS_H + (unsigned)((bF * 8 + cF) * 16 + (l >> 3)) * 128u + (unsigned)(s * 8 + n7) * 4u;
        unsigned t, cnt;
        asm volatile("s_movk_i32 %[cnt], " SPINS "\n"
                     "1:\n\t"
                     "buffer_load_dword %[h0], %[o], %[srd], 0 offen sc1\n\t"
                     "buffer_load_dword %[h1], %[o], %[srd], 0 offen offset:1024 sc1\n\t"
                     "s_sub_u32 %[cnt], %[cnt], 1\n\t"
                     "s_waitcnt vmcnt(0)\n\t"
                     "s_cbranch_scc1 2f\n\t"
                     "v_max_u32 %[t], %[h0], %[h1]\n\t"
                     "v_cmp_eq_u32 vcc, -1, %[t]\n\t"
                     "s_cbranch_vccnz 1b\n"
                     "2:"
                     : [t] "=&v"(t), [cnt] "=&s"(cnt), [h0] "=&v"(h0), [h1] "=&v"(h1) : [o] "v"(ho), [srd] "s"(k.s_ws) : "memory", "vcc", "scc");
    }
    float a = mix32(bf_lo(h0) + bf_lo(h1), bf_hi(h0) + bf_hi(h1));
    a += dppf<XOR8>(a);
    float hh = rb(x16_sum(a));
    unsigned yo = WS_Y + (unsigned)(bF * 16 + cF * 2 + (s >> 1)) * 7168u + (unsigned)(l >> 3) * 128u + (unsigned)((s & 1) * 64 + n7 * 8), yo2 = yo + 4096u;
    const v4i s_out = mksrd(out, 2048);
    unsigned oo = (l & 24) ? OOB : (unsigned)(bF * 512 + col) * 2u + (unsigned)(l >> 5) * 32u;
    PIN(hh); PIN(yo); PIN(yo2); PIN(oo);
    ST(7);
    v2f y0, y1, y2, y3, y4, y5, y6;
    unsigned t, cnt;
    asm volatile("s_movk_i32 %[cnt], " SPINS "\n"
                 "1:\n\t"
                 "buffer_load_dwordx2 v[160:161], %[o1], %[srd], 0 offen sc1\n\t"
                 "buffer_load_dwordx2 v[162:163], %[o1], %[srd], 0 offen offset:1024 sc1\n\t"
                 "buffer_load_dwordx2 v[164:165], %[o1], %[srd], 0 offen offset:2048 sc1\n\t"
                 "buffer_load_dwordx2 v[166:167], %[o1], %[srd], 0 offen offset:3072 sc1\n\t"
                 "buffer_load_dwordx2 v[168:169], %[o2], %[srd], 0 offen sc1\n\t"
                 "buffer_load_dwordx2 v[170:171], %[o2], %[srd], 0 offen offset:1024 sc1\n\t"
                 "buffer_load_dwordx2 v[172:173], %[o2], %[srd], 0 offen offset:2048 sc1\n\t"
                 "s_sub_u32 %[cnt], %[cnt], 1\n\t"
                 "s_waitcnt vmcnt(4)\n\t"
                 "v_max3_u32 %[t], v161, v163, v165\n\t"
                 "s_waitcnt vmcnt(2)\n\t"
                 "v_max3_u32 %[t], %[t], v167, v169\n\t"
                 "s_waitcnt vmcnt(0)\n\t"
                 "s_cbranch_scc1 2f\n\t"
                 "v_max3_u32 %[t], %[t], v171, v173\n\t"
                 "v_cmp_eq_u32 vcc, -1, %[t]\n\t"
                 "s_cbranch_vccnz 1b\n"
                 "2:"
                 : [t] "=&v"(t), [cnt] "=&s"(cnt), "=&{v[160:161]}"(y0), "=&{v[162:163]}"(y1), "=&{v[164:165]}"(y2),
                   "=&{v[166:167]}"(y3), "=&{v[168:169]}"(y4), "=&{v[170:171]}"(y5), "=&{v[172:173]}"(y6)
                 : [o1] "v"(yo), [o2] "v"(yo2), [srd] "s"(k.s_ws) : "memory", "vcc", "scc");
    ST(8);
    const v2f ys = ((y0 + y1) + (y2 + y3)) + ((y4 + y5) + y6);
    float y = mix32(ys.x, ys.y);
    y += dppf<XOR8>(y);
    y = x16_sum(y);
    const unsigned r = cvtpk(hh + rb(y), 0.f);
    asm volatile("buffer_store_short %0, %1, %2, 0 offen" ::"v"(r), "v"(oo), "s"(s_out) : "memory");
    if ((bF | cF | s) == 0) {
        const unsigned zo = l ? OOB : 0u;
        asm volatile("buffer_store_dword %0, %1, %2, 0 offen sc1" ::"v"(k.tagv), "v"(zo), "s"(k.s_ws) : "memory");
    }
    ST(9);
}

__global__ void __launch_bounds__(512, 2)
fused_block(const u16* __restrict__ x, const u16* __restrict__ kc, const u16* __restrict__ vc, const u16* __restrict__ wq,
            const u16* __restrict__ wo, const u16* __restrict__ w1, const u16* __restrict__ w2, u16* __restrict__ out,
            unsigned char* __restrict__ ws) {
    __shared__ __attribute__((aligned(1024))) unsigned char lds_raw[L_END];
    asm volatile("" ::"v"((LAS unsigned char*)lds_raw));
    const int tid = threadIdx.x, l = tid & 63;
    const int w = __builtin_amdgcn_readfirstlane(tid >> 6);
    const int xc = blockIdx.x & 7, u = blockIdx.x >> 3;
    const int gq = l >> 4, c = l & 15;
#define QM 0x65432107
    const int kq = u & 3, cq = u >> 2, gQ = (QM >> (xc * 4)) & 7;
    const int bA = u >> 4, gA = (xc + 7) & 7, s = u & 15;
#define XR 0x06543217
    const int xr = (XR >> (xc * 4)) & 7;
    const bool prj = xr < 4, mlp = s < 14;
    const int gP = xr * 2 + ((u >> 3) & 1), cb = u & 7, dh = u >> 4;
    const int fb = (xr - 4) * 14 + (s < 14 ? s : 13);
    Ctx k;
    k.l = l; k.w = w; k.gq = gq; k.c = c;
    k.s_ws = mksrd(ws, WS_END); k.s_x = mksrd(x, 2048);
    unsigned ev;
    asm volatile("s_load_dword %0, %1, 0x0" : "=s"(ev) : "s"(ws), "s"(kc), "s"(vc), "s"(wq), "s"(wo), "s"(w1), "s"(w2), "s"(out) : "memory");

    unsigned xv; v4u xa;
    {
        const unsigned xo = (unsigned)((w >> 1) & 1) * 1024u + (unsigned)l * 16u, xvo = (unsigned)(((tid >> 7) & 1) * 512 + kq * 128 + (tid & 127)) * 2u;
        asm volatile("buffer_load_dwordx4 %0, %2, %3, 0 offen\n\t"
                     "buffer_load_ushort %1, %4, %3, 0 offen"
                     : "=&{v[160:163]}"(xa), "=&{v164}"(xv) : "v"(xo), "s"(k.s_x), "v"(xvo) : "memory");
    }
    k.p8 = (unsigned)(((l & 7) ^ (((l >> 4) & 1) << 1)) * 16);
    {
        const v4i s_wq = mksrd(wq, 4u << 20);
        const unsigned vo = (unsigned)(l >> 3) * 8192u + k.p8;
        const unsigned so = (unsigned)(kq * 128 + w * 16) * 8192u + (unsigned)(gQ * 512 + cq * 64) * 2u;
        dma<NTA>(L_C + w * 2048, vo, s_wq, so);
        dma<NTA>(L_C + w * 2048 + 1024, vo ^ 64u, s_wq, so + 8u * 8192u);
    }
    if (w == 5) {
        const unsigned c0 = WS_O + (unsigned)(bA * 8 + gA) * 16384u + (unsigned)s * 128u + (unsigned)(l >> 3) * 2048u + (unsigned)(l & 7) * 16u;
        const unsigned c1 = l < 56 ? WS_Y + blockIdx.x * 896u + (unsigned)l * 16u : OOB;
        const unsigned c2 = (u < 4 ? WS_H + (unsigned)(gA * 4 + u) * 1024u : OOB) + (unsigned)l * 16u;
        const v4u m1 = {~0u, ~0u, ~0u, ~0u};
        asm volatile("buffer_store_dwordx4 %0, %1, %4, 0 offen sc1\n\t"
                     "buffer_store_dwordx4 %0, %2, %4, 0 offen sc1\n\t"
                     "buffer_store_dwordx4 %0, %3, %4, 0 offen sc1\n\ts_nop 1" ::"v"(m1), "v"(c0), "v"(c1), "v"(c2), "s"(k.s_ws) : "memory");
    }
#define WAIT5(n) "s_cmp_eq_u32 %[w], 5\n\ts_cbranch_scc1 1f\n\ts_waitcnt vmcnt(" #n ")\n1:\n\ts_waitcnt vmcnt(" #n " + 3)"
    const v4i s_k = mksrd(kc, 32u << 20), s_v = mksrd(vc, 32u << 20);
    const unsigned kvb = (unsigned)((bA * 8 + gA) * 8192 + 6144 + s * 128 + w * 16) * 256u;
    unsigned vvo = (unsigned)gq * 256u + (unsigned)((c ^ (gq << 1)) * 16);
    unsigned kvo = (unsigned)gq * 256u + (unsigned)((c ^ gq) * 16);
#define KLD(i) dma<NTA>(L_K + (w * 4 + (i)) * 1024, kvo ^ (unsigned)((i) * 64), s_k, kvb + (i) * 1024)
#define VLD(i) dma<NTA>(L_V + (w * 4 + (i)) * 1024, vvo ^ (unsigned)(((i) & 1) * 128), s_v, kvb + (i) * 1024)
    k.q4 = (unsigned)((l >> 2) & 3); k.p4 = (unsigned)(l & 3);
    k.trsw = (k.q4 >> 1) | ((unsigned)(gq & 1) << 1);
    k.tr128 = (unsigned)(gq * 8 + k.q4) * 128u + k.p4 * 8u;
    unsigned qa = L_C + k.tr128 + (((unsigned)w ^ k.trsw) * 32u);
    unsigned la = L_LN + (unsigned)((c >> 2) & 1) * 320u + (unsigned)gq * 16u;
    unsigned qoff = (l < 32 && w < 4) ? WS_Q + (unsigned)((((gQ * 2 + gq) * 4 + kq) * 512) + cq * 64 + w * 16 + c) * 8u
                  : tid == 320 ? WS_F + (unsigned)(gA * 32 + u) * 4u : OOB;
    PIN(qa); PIN(la); PIN(qoff);
    asm volatile(WAIT5(2) : "+{v[160:163]}"(xa), "+{v164}"(xv) : [w] "s"(w) : "memory", "scc");
    if (w < 4) {
        const float s1 = dot2(xa.w, 0x3f803f80u, dot2(xa.z, 0x3f803f80u, dot2(xa.y, 0x3f803f80u, dot2(xa.x, 0x3f803f80u, 0.f))));
        const float s2 = dot2(xa.w, xa.w, dot2(xa.z, xa.z, dot2(xa.y, xa.y, dot2(xa.x, xa.x, 0.f))));
        const float t = x16_sum(sum16(mix32(s1, s2))) * (1.0f / 512.0f);
        const float mean = __builtin_bit_cast(float, __builtin_amdgcn_readlane(__builtin_bit_cast(int, t), 0));
        const float msq = __builtin_bit_cast(float, __builtin_amdgcn_readlane(__builtin_bit_cast(int, t), 32));
        const float var = vmax(msq - mean * mean, 0.f);
        const float mr = rb(mean), den = rb(__builtin_amdgcn_sqrtf(rb(rb(var) + 1e-5f)));
        const float ln = __fdiv_rn(rb(bf_lo(xv) - mr), den);
        lds_st<u16>(L_LN + (tid >> 7) * 320 + (tid & 127) * 2, (u16)cvtpk(ln, ln));
    } else if (w == 4) {
        lds_st<unsigned>(L_Z + l * 4, l < 48 ? 0u : 0xff800000u);
    } else if (w < 7) {
        lds_st<v4u>(L_X + (unsigned)(w - 5) * 1024u + (unsigned)l * 16u, xa);
    }
    asm volatile(WAIT5(1) :: [w] "s"(w) : "memory", "scc");
    KLD(0);
    asm volatile(WAIT5(1) :: [w] "s"(w) : "memory", "scc");
    asm volatile("s_waitcnt lgkmcnt(0)\n\ts_barrier" : "+s"(ev) :: "memory");
    KLD(1);
    ST(1);
    k.tag = ev + 1u;
    k.tagv = k.tag;
    PIN(k.tagv);
    {
        if (w < 4) {
            const v4f zz = {0.f, 0.f, 0.f, 0.f};
            v4u bw[4], al[4];
#pragma unroll
            for (int ks = 0; ks < 4; ++ks) { bw[ks] = frag_tr(qa + ks * 4096, qa + ks * 4096 + 512); al[ks] = lds_ld<v4u>(la + ks * 64); }
            __builtin_amdgcn_sched_barrier(0);
            v4f acc = mfma(al[0], bw[0], zz);
#pragma unroll
            for (int ks = 1; ks < 4; ++ks) acc = mfma(al[ks], bw[ks], acc);
            put8(k.s_ws, qoff, __float_as_uint(acc.x), k.tagv);
        } else {
            asm volatile("s_waitcnt vmcnt(2)\n\tbuffer_store_dword %0, %1, %2, 0 offen sc1" ::"v"(k.tagv), "v"(qoff), "s"(k.s_ws) : "memory");
        }
    }
    ST(2);

    {
        unsigned qo = WS_Q + (unsigned)(gA * 2 + bA) * 16384u + (unsigned)(l >> 5) * 8192u + (unsigned)w * 512u + (unsigned)(l & 31) * 16u;
        PIN(qo);
        WAITV(2);
        KLD(2);
        WAITV(2);
        KLD(3);
        unsigned qw = L_QI + (unsigned)(w >> 1) * 320u + (unsigned)(w & 1) * 128u + (unsigned)(l & 31) * 4u + (unsigned)(l >> 5) * 2u;
        unsigned aq = L_QI + (unsigned)(c >> 2) * 320u + (unsigned)gq * 16u;
        unsigned mxa = L_MX + (unsigned)gq * 16u;
        unsigned mxw = mxa + (unsigned)(c & 3) * 4u;
        unsigned pr = L_P + (unsigned)gq * 64u + (unsigned)(c & 3) * 16u;
        unsigned pw = L_P + (unsigned)(w >> 1) * 256u + (unsigned)(c >> 2) * 64u + (unsigned)gq * 16u + (unsigned)((w & 1) * 4 + (c & 3)) * 2u;
        const unsigned vrow = (unsigned)gq * 4u + k.q4;
        unsigned va = L_V + vrow * 256u + k.p4 * 8u + (((unsigned)w ^ (vrow & 7u)) * 32u);
        unsigned oo = l < 16 ? WS_O + (unsigned)(bA * 8 + gA) * 16384u + (unsigned)w * 2048u + (unsigned)s * 128u + (unsigned)l * 8u : OOB;
        unsigned ck = WS_F + ((prj && l < 32) ? (unsigned)gP * 128u + (unsigned)l * 4u : (unsigned)(l & 7) * 128u + (unsigned)((l >> 3) & 3) * 4u);
        PIN(qw); PIN(aq); PIN(mxa); PIN(mxw); PIN(pr); PIN(pw); PIN(va); PIN(oo); PIN(ck);
        unsigned vvo1 = vvo ^ 128u;
        PIN(vvo); PIN(vvo1);
        WAITV(1);
        ST(3);
        v4u g0, g1;
        const unsigned k4 = 4096u;
        asm volatile("buffer_load_dwordx4 %0, %2, %3, 0 offen sc1\n\t"
                     "buffer_load_dwordx4 %1, %2, %3, %4 offen sc1"
                     : "=&{v[160:163]}"(g0), "=&{v[164:167]}"(g1) : "v"(qo), "s"(k.s_ws), "s"(k4) : "memory");
        VLD(0);
        WAITV(3);
        VLD(1);
        v4u kf[4];
#pragma unroll
        for (int st = 0; st < 4; ++st) kf[st] = lds_ld<v4u>(L_K + (unsigned)(w * 16 + c) * 256u + (unsigned)(((st * 4 + gq) ^ c) * 16));
        {
            unsigned t, t2, cnt;
#define NTS " nt"
            asm volatile("s_movk_i32 %[cnt], " SPINS "\n\t"
                         "s_mov_b32 m0, %[m2]\n\t"
                         "s_waitcnt vmcnt(2)\n\t"
                         "v_bitop3_b32 %[t], v161, v163, %[tag] bitop3:0x7e\n\t"
                         "v_bitop3_b32 %[t2], v165, v167, %[tag] bitop3:0x7e\n\t"
                         "v_or_b32 %[t], %[t], %[t2]\n\t"
                         "v_cmp_ne_u32 vcc, 0, %[t]\n\t"
                         "s_cbranch_vccz 2f\n\t"
                         "buffer_load_dwordx4 v[160:163], %[o1], %[srd], 0 offen sc1\n\t"
                         "buffer_load_dwordx4 v[164:167], %[o1], %[srd], %[k4] offen sc1\n\t"
                         "s_sub_u32 %[cnt], %[cnt], 1\n"
                         "2:\n\t"
                         "buffer_load_dwordx4 %[v2], %[sv], %[kvb] offen offset:2048" NTS " lds\n\t"
                         "buffer_load_dwordx4 %[v3], %[sv], %[kvb] offen offset:3072" NTS " lds\n\t"
                         "s_cbranch_vccz 3f\n\t"
                         "s_waitcnt vmcnt(2)\n"
                         "1:\n\t"
                         "v_bitop3_b32 %[t], v161, v163, %[tag] bitop3:0x7e\n\t"
                         "v_bitop3_b32 %[t2], v165, v167, %[tag] bitop3:0x7e\n\t"
                         "v_or_b32 %[t], %[t], %[t2]\n\t"
                         "v_cmp_ne_u32 vcc, 0, %[t]\n\t"
                         "s_cbranch_vccz 3f\n\t"
                         "buffer_load_dwordx4 v[160:163], %[o1], %[srd], 0 offen sc1\n\t"
                         "buffer_load_dwordx4 v[164:167], %[o1], %[srd], %[k4] offen sc1\n\t"
                         "s_sub_u32 %[cnt], %[cnt], 1\n\t"
                         "s_waitcnt vmcnt(0)\n\t"
                         "s_cbranch_scc0 1b\n"
                         "3:"
                         : [t] "=&v"(t), [t2] "=&v"(t2), [cnt] "=&s"(cnt), "+{v[160:163]}"(g0), "+{v[164:167]}"(g1)
                         : [o1] "v"(qo), [srd] "s"(k.s_ws), [k4] "s"(k4), [tag] "s"(k.tag), [m2] "s"(L_V + w * 4096), [v2] "v"(vvo), [v3] "v"(vvo1),
                           [sv] "s"(s_v), [kvb] "s"(kvb)
                         : "memory", "vcc", "scc");
            const float qv = mix32(__uint_as_float(g0.x) + __uint_as_float(g1.x), __uint_as_float(g0.z) + __uint_as_float(g1.z));
            lds_st<u16>(qw, (u16)cvtpk(qv, qv));
        }
        BAR();
        ST(4);
        float s0;
        {
            v4u qf[4];
#pragma unroll
            for (int st = 0; st < 4; ++st) qf[st] = lds_ld<v4u>(aq + st * 64);
            __builtin_amdgcn_sched_barrier(0);
            v4f sc = {0.f, 0.f, 0.f, 0.f};
#pragma unroll
            for (int st = 0; st < 4; ++st) sc = mfma(qf[st], kf[st], sc);
            s0 = rb(rb(sc.x) * 0.08838834764831845f);
            __builtin_amdgcn_ds_fmaxf((LAS float*)(size_t)mxw, s0, 0, 0, false);
        }
        BAR();
        {
            const v4f m4 = lds_ld<v4f>(mxa);
            const float mx = vmax3(m4.x, m4.y, vmax(m4.z, m4.w));
            const float e0 = __builtin_amdgcn_exp2f((s0 - mx) * 1.4426950408889634f);
            lds_st<u16>(pw, (u16)cvtpk(e0, e0));
        }
        BAR();
        v4u pf[4];
#pragma unroll
        for (int j = 0; j < 4; ++j) pf[j] = lds_ld<v4u>(pr + j * 256);
        unsigned ct;
        asm volatile("s_cmp_lg_u32 %1, 6\n\ts_cbranch_scc1 1f\n\tbuffer_load_dword %0, %2, %3, 0 offen sc1\n1:"
                     : "=&{v168}"(ct) : "s"(w), "v"(ck), "s"(k.s_ws) : "memory", "scc");
        if (w == 7) {
            const v4f m4 = lds_ld<v4f>(L_MX + (unsigned)(c & 3) * 16u);
            const float mx = vmax3(m4.x, m4.y, vmax(m4.z, m4.w));
            const v4u ones = {0x3f803f80u, 0x3f803f80u, 0x3f803f80u, 0x3f803f80u};
            v4f ls = {0.f, 0.f, 0.f, 0.f};
#pragma unroll
            for (int j = 0; j < 4; ++j) ls = mfma(ones, pf[j], ls);
            put16(k.s_ws, l < 8 ? WS_ML + (unsigned)((bA * 8 + gA) * 16 + s) * 128u + (unsigned)l * 16u : OOB, __float_as_uint(mx), __float_as_uint(ls.x), k.tagv);
        }
        ST(12);
        if (prj) {
            prj_load<0, REP>(k, gP, dh, cb, wo);
            asm volatile("s_cmp_gt_u32 %0, 5\n\ts_cbranch_scc1 1f\n\ts_waitcnt vmcnt(%1)\n1:\n\ts_waitcnt vmcnt(%2)" :: "s"(w), "n"(REP), "n"(REP + 1) : "memory", "scc");
        } else {
            mlp_load<0, REM>(k, fb, w1, w2);
            asm volatile("s_cmp_gt_u32 %0, 5\n\ts_cbranch_scc1 1f\n\ts_waitcnt vmcnt(%1)\n1:\n\ts_waitcnt vmcnt(%2)" :: "s"(w), "n"(REM), "n"(REM + 1) : "memory", "scc");
        }
        asm volatile("s_barrier" ::: "memory");
        ST(5);
        {
            v4u vf[4];
#pragma unroll
            for (int j = 0; j < 4; ++j) vf[j] = frag_tr(va + j * 8192, va + j * 8192 + 4096);
            __builtin_amdgcn_sched_barrier(0);
            v4f o = {0.f, 0.f, 0.f, 0.f};
#pragma unroll
            for (int j = 0; j < 4; ++j) o = mfma(pf[j], vf[j], o);
            put8(k.s_ws, oo, cvtpk(o.x, o.y), cvtpk(o.z, o.w));
        }
        {
            unsigned cnt;
            asm volatile("s_cmp_lg_u32 %[w], 6\n\t"
                         "s_cbranch_scc1 3f\n\t"
                         "s_movk_i32 %[cnt], " SPINS "\n\t"
                         "s_cmp_lt_u32 %[pj], 4\n\t"
                         "s_cbranch_scc1 4f\n\t"
                         "s_waitcnt vmcnt(%[nm])\n\t"
                         "s_branch 1f\n"
                         "4:\n\t"
                         "s_waitcnt vmcnt(%[np])\n"
                         "1:\n\t"
                         "v_cmp_ne_u32 vcc, %[tag], v168\n\t"
                         "s_cbranch_vccz 3f\n\t"
                         "buffer_load_dword v168, %[ck], %[srd], 0 offen sc1\n\t"
                         "s_sub_u32 %[cnt], %[cnt], 1\n\t"
                         "s_waitcnt vmcnt(0)\n\t"
                         "s_cbranch_scc0 1b\n"
                         "3:\n\t"
                         "s_barrier"
                         : [cnt] "=&s"(cnt), "+{v168}"(ct)
                         : [w] "s"(w), [pj] "s"(xr), [nm] "n"(REM + 1), [np] "n"(REP + 1), [tag] "s"(k.tag), [ck] "v"(ck), [srd] "s"(k.s_ws)
                         : "memory", "vcc", "scc");
        }
        ST(6);
    }
    if (!prj) {
        if (mlp) {
            if (MD) __builtin_amdgcn_s_sleep(MD);
            mlp_load<REM, 4>(k, fb, w1, w2);
            mlp_run(STPASS k, bA, fb, w2);
        }
    } else {
        prj_load<REP, 4>(k, gP, dh, cb, wo);
        const int bP = w >> 2;
        unsigned oo = WS_O + (unsigned)(bP * 8 + gP) * 16384u + (unsigned)(dh * 4 + (w & 3)) * 2048u + (unsigned)l * 8u;
        unsigned mo = WS_ML + (unsigned)((bP * 8 + gP) * 16 + (c >> 2) * 4 + gq) * 128u + (unsigned)(c & 3) * 16u;
        PIN(oo); PIN(mo);
        v4u ml;
        v2u o0, o1, o2, o3;
        if (PW < 63) WAITV(PW);
#define OLDS "buffer_load_dwordx2 v[160:161], %[off], %[srd], 0 offen sc1\n\t"             \
             "buffer_load_dwordx2 v[162:163], %[off], %[srd], 0 offen offset:512 sc1\n\t"  \
             "buffer_load_dwordx2 v[164:165], %[off], %[srd], 0 offen offset:1024 sc1\n\t" \
             "buffer_load_dwordx2 v[166:167], %[off], %[srd], 0 offen offset:1536 sc1\n\t"
        asm volatile("buffer_load_dwordx4 v[176:179], %[mo], %[srd], 0 offen sc1\n\t" OLDS
                     : "=&{v[160:161]}"(o0), "=&{v[162:163]}"(o1), "=&{v[164:165]}"(o2), "=&{v[166:167]}"(o3), "=&{v[176:179]}"(ml)
                     : [off] "v"(oo), [mo] "v"(mo), [srd] "s"(k.s_ws) : "memory");
        unsigned ow = L_OS + (unsigned)w * 128u + (unsigned)l * 2u;
        unsigned ar[4];
#pragma unroll
        for (int t = 0; t < 4; ++t) { ar[t] = (c & 3) == t ? L_OS + (unsigned)w * 128u + (unsigned)gq * 16u : L_Z; PIN(ar[t]); }
        unsigned hw = L_HR + (unsigned)w * 256u + (unsigned)c * 16u;
        unsigned hr = hw + (unsigned)gq * 256u;
        unsigned hoff = (gq & 1) ? OOB : WS_H + (unsigned)((bP * 8 + cb) * 16 + gP * 2 + dh) * 128u + (unsigned)(gq >> 1) * 64u + (unsigned)c * 4u;
        v4f acc;
        {
            const unsigned xr = (((gP | dh | (w & 3)) == 0) ? L_X + (unsigned)bP * 1024u + (unsigned)cb * 128u : L_Z) + (unsigned)c * 2u;
            acc.x = bf_lo(lds_ld<u16>(xr)); acc.y = bf_lo(lds_ld<u16>(xr + 32)); acc.z = bf_lo(lds_ld<u16>(xr + 64)); acc.w = bf_lo(lds_ld<u16>(xr + 96));
        }
        PIN(ow); PIN(hw); PIN(hr); PIN(hoff); PIN(acc);
        asm volatile("s_waitcnt vmcnt(5)\n\ts_barrier" ::: "memory");
        if (w != 2) ST(7);
        v4u wf[4][2];
        {
            const unsigned a = L_R + (unsigned)(w & 3) * 8192u + k.tr128;
#pragma unroll
            for (int t = 0; t < 4; ++t)
#pragma unroll
                for (int ks = 0; ks < 2; ++ks) { const unsigned q = a + (((unsigned)t ^ k.trsw) * 32u) + ks * 4096; wf[t][ks] = frag_tr(q, q + 512); }
        }
        {
            unsigned t, cnt;
            asm volatile("s_movk_i32 %[cnt], " SPINS "\n"
                         "1:\n\t"
                         "s_waitcnt vmcnt(4)\n\t"
                         "v_bitop3_b32 %[t], v177, v179, %[tag] bitop3:0x7e\n\t"
                         "v_cmp_ne_u32 vcc, 0, %[t]\n\t"
                         "s_cbranch_vccz 2f\n\t"
                         "s_sub_u32 %[cnt], %[cnt], 1\n\t"
                         "s_cbranch_scc1 2f\n\t"
                         "buffer_load_dwordx4 v[176:179], %[mo], %[srd], 0 offen sc1\n\t" OLDS
                         "s_branch 1b\n"
                         "2:\n\t"
                         "s_waitcnt vmcnt(0)\n\t"
                         "v_max3_u32 %[t], v161, v163, v165\n\t"
                         "v_max_u32 %[t], %[t], v167\n\t"
                         "v_cmp_eq_u32 vcc, -1, %[t]\n\t"
                         "s_cbranch_vccz 3f\n\t" OLDS
                         "3:"
                         : [t] "=&v"(t), [cnt] "=&s"(cnt), "+{v[160:161]}"(o0), "+{v[162:163]}"(o1), "+{v[164:165]}"(o2), "+{v[166:167]}"(o3), "+{v[176:179]}"(ml)
                         : [off] "v"(oo), [mo] "v"(mo), [srd] "s"(k.s_ws), [tag] "s"(k.tag) : "memory", "vcc", "scc");
        }
        ST(13);
        float wt;
        {
            const float m = __uint_as_float(ml.x);
            float M = vmax(m, dppf<XOR8>(m));
            M = x32_max(x16_max(vmax(M, dppf<0x124>(M))));
            const float f = __builtin_amdgcn_exp2f((m - M) * 1.4426950408889634f);
            float L = f * __uint_as_float(ml.z);
            L += dppf<XOR8>(L);
            L += dppf<0x124>(L);
            L = x32_sum(x16_sum(L));
            wt = f * __builtin_amdgcn_rcpf(L);
        }
#define WB(n) dppf<0x150 + (n)>(wt)
        v2f W0 = {WB(0), WB(1)}, W1 = {WB(2), WB(3)}, W2 = {WB(4), WB(5)}, W3 = {WB(6), WB(7)};
        v2f W4 = {WB(8), WB(9)}, W5 = {WB(10), WB(11)}, W6 = {WB(12), WB(13)}, W7 = {WB(14), WB(15)};
        PIN(W0); PIN(W1); PIN(W2); PIN(W3); PIN(W4); PIN(W5); PIN(W6); PIN(W7);
#pragma unroll
        for (int t = 0; t < 4; ++t) { PIN(wf[t][0]); PIN(wf[t][1]); }
        {
            unsigned t, cnt;
            asm volatile("s_movk_i32 %[cnt], " SPINS "\n"
                         "1:\n\t"
                         "s_waitcnt vmcnt(0)\n\t"
                         "v_max3_u32 %[t], v161, v163, v165\n\t"
                         "v_max_u32 %[t], %[t], v167\n\t"
                         "v_cmp_eq_u32 vcc, -1, %[t]\n\t"
                         "s_cbranch_vccz 2f\n\t"
                         "s_sub_u32 %[cnt], %[cnt], 1\n\t"
                         "s_cbranch_scc1 2f\n\t" OLDS
                         "s_branch 1b\n"
                         "2:"
                         : [t] "=&v"(t), [cnt] "=&s"(cnt), "+{v[160:161]}"(o0), "+{v[162:163]}"(o1), "+{v[164:165]}"(o2), "+{v[166:167]}"(o3)
                         : [off] "v"(oo), [srd] "s"(k.s_ws) : "memory", "vcc", "scc");
            ST(15);
#define UP(v) (v2f){bf_lo(v), bf_hi(v)}
            v2f a01 = UP(o0.x) * W0, a23 = UP(o0.y) * W1;
            a01 = __builtin_elementwise_fma(UP(o1.x), W2, a01); a23 = __builtin_elementwise_fma(UP(o1.y), W3, a23);
            a01 = __builtin_elementwise_fma(UP(o2.x), W4, a01); a23 = __builtin_elementwise_fma(UP(o2.y), W5, a23);
            a01 = __builtin_elementwise_fma(UP(o3.x), W6, a01); a23 = __builtin_elementwise_fma(UP(o3.y), W7, a23);
            const float v0 = a01.x, v1 = a01.y, v2 = a23.x, v3 = a23.y;
            const float oc = mix16(mix32(v0, v2), mix32(v1, v3));
            lds_st<u16>(ow, (u16)cvtpk(oc, oc));
            v4u af[4][2];
#pragma unroll
            for (int t = 0; t < 4; ++t) { af[t][0] = lds_ld<v4u>(ar[t]); af[t][1] = lds_ld<v4u>(ar[t] + 64); }
            __builtin_amdgcn_sched_barrier(0);
#pragma unroll
            for (int t = 0; t < 4; ++t) { acc = mfma(af[t][0], wf[t][0], acc); acc = mfma(af[t][1], wf[t][1], acc); }
            lds_st<v4f>(hw, acc);
        }
        BAR();
        if ((w & 3) == 0) {
            const v4f t = lds_ld<v4f>(hr);
            const float a = x16_sum(mix32(t.x, t.z)), bq = x16_sum(mix32(t.y, t.w));
            const unsigned hv = cvtpk(a, bq);
            asm volatile("buffer_store_dword %0, %1, %2, 0 offen sc1" ::"v"(hv), "v"(hoff), "s"(k.s_ws) : "memory");
            ST(14);
        }
        if (w == 2 && (u & 1) == 0) fin_run(STPASS k, xr >> 1, (xr & 1) * 4 + (u >> 3), (u >> 1) & 3, out);
    }
}

extern "C" void kernel_launch(void* const* d_in, const int* in_sizes, int n_in, void* d_out, int out_size, void* d_ws, size_t ws_size,
                              hipStream_t stream) {
    fused_block<<<256, 512, 0, stream>>>((const u16*)d_in[0], (const u16*)d_in[1], (const u16*)d_in[2], (const u16*)d_in[3], (const u16*)d_in[4],
                                         (const u16*)d_in[6], (const u16*)d_in[7], (u16*)d_out, (unsigned char*)d_ws);
}
```

```cpp
#include <hip/hip_runtime.h>
#include <hip/hip_bf16.h>

typedef unsigned short u16;
typedef unsigned long long u64;
typedef unsigned v4u __attribute__((ext_vector_type(4)));
typedef unsigned v2u __attribute__((ext_vector_type(2)));
typedef int v4i __attribute__((ext_vector_type(4)));
typedef float v4f __attribute__((ext_vector_type(4)));
typedef float v2f __attribute__((ext_vector_type(2)));
typedef short v4s __attribute__((ext_vector_type(4)));
typedef __bf16 v8bf __attribute__((ext_vector_type(8)));
typedef __bf16 v2bf __attribute__((ext_vector_type(2)));
#define LAS __attribute__((address_space(3)))
#define DEV __device__ __forceinline__

#define REP 2
#define REM 1
#define MD 0
#define PW 0
#define NTA 1
#define NTW 1

constexpr unsigned WS_F = 1024;
constexpr unsigned WS_Q = 4096;
constexpr unsigned WS_ML = WS_Q + 262144;
constexpr unsigned WS_O = WS_ML + 32768;
constexpr unsigned WS_H = WS_O + 262144;
constexpr unsigned WS_Y = WS_H + 32768;
constexpr unsigned WS_END = WS_Y + 229376;

constexpr unsigned L_K = 0;
constexpr unsigned L_V = 32768;
constexpr unsigned L_R = 65536;
constexpr unsigned L_C = 131072;
constexpr unsigned L_LN = 147456;
constexpr unsigned L_QI = L_LN + 640;
constexpr unsigned L_Z = L_QI + 1280;
constexpr unsigned L_MX = L_Z + 192;
constexpr unsigned L_P = L_MX + 64;
constexpr unsigned L_OS = L_P + 1024;
constexpr unsigned L_HR = L_OS + 1024;
constexpr unsigned L_ZR = L_HR + 2048;
constexpr unsigned L_GS = L_ZR + 2560;
constexpr unsigned L_ZE = L_GS + 128;
constexpr unsigned L_X = L_ZE + 1024;
constexpr unsigned L_END = L_X + 2048;

DEV float bf_lo(unsigned v) { return __uint_as_float(v << 16); }
DEV float bf_hi(unsigned v) { return __uint_as_float(v & 0xffff0000u); }
DEV unsigned cvtpk(float a, float b) { v2f t = {a, b}; return __builtin_bit_cast(unsigned, __builtin_convertvector(t, v2bf)); }
DEV float rb(float a) { return bf_lo(cvtpk(a, a)); }
DEV float vmax(float a, float b) { return __builtin_elementwise_maximum(a, b); }
DEV float vmax3(float a, float b, float c) { return vmax(a, vmax(b, c)); }
template <int CTRL> DEV float dppf(float x) { return __builtin_bit_cast(float, __builtin_amdgcn_mov_dpp(__builtin_bit_cast(int, x), CTRL, 0xf, 0xf, true)); }
template <int CTRL> DEV unsigned dppu(unsigned x) { return (unsigned)__builtin_amdgcn_mov_dpp((int)x, CTRL, 0xf, 0xf, true); }
constexpr int XOR1 = 0xB1, XOR2 = 0x4E, XOR7 = 0x141, XOR8 = 0x128;
DEV float mix16(float a, float b) { auto s = __builtin_amdgcn_permlane16_swap(__float_as_uint(a), __float_as_uint(b), false, false); return __uint_as_float(s[0]) + __uint_as_float(s[1]); }
DEV float mix32(float a, float b) { auto s = __builtin_amdgcn_permlane32_swap(__float_as_uint(a), __float_as_uint(b), false, false); return __uint_as_float(s[0]) + __uint_as_float(s[1]); }
DEV float x16_sum(float x) { return mix16(x, x); }
DEV float x32_sum(float x) { return mix32(x, x); }
DEV float x16_max(float x) { auto s = __builtin_amdgcn_permlane16_swap(__float_as_uint(x), __float_as_uint(x), false, false); return vmax(__uint_as_float(s[0]), __uint_as_float(s[1])); }
DEV float x32_max(float x) { auto s = __builtin_amdgcn_permlane32_swap(__float_as_uint(x), __float_as_uint(x), false, false); return vmax(__uint_as_float(s[0]), __uint_as_float(s[1])); }
DEV float sum8(float x) { x += dppf<XOR1>(x); x += dppf<XOR2>(x); x += dppf<XOR7>(x); return x; }
DEV float sum16(float x) { x = sum8(x); x += dppf<XOR8>(x); return x; }
DEV float dot2(unsigned a, unsigned b, float c) { return __builtin_amdgcn_fdot2_f32_bf16(__builtin_bit_cast(v2bf, a), __builtin_bit_cast(v2bf, b), c, false); }
DEV v4f mfma(v4u a, v4u b, v4f c) { return __builtin_amdgcn_mfma_f32_16x16x32_bf16(__builtin_bit_cast(v8bf, a), __builtin_bit_cast(v8bf, b), c, 0, 0, 0); }
DEV v4i mksrd(const void* p, unsigned n) { u64 a = (u64)p; v4i r; r.x = (int)(unsigned)a; r.y = (int)(unsigned)(a >> 32); r.z = (int)n; r.w = 0x00020000; return r; }

template <typename T> DEV T lds_ld(unsigned a) { return *(LAS T*)(size_t)a; }
template <typename T> DEV void lds_st(unsigned a, T v) { *(LAS T*)(size_t)a = v; }
DEV v2u lds_tr(unsigned a) { return __builtin_bit_cast(v2u, __builtin_amdgcn_ds_read_tr16_b64_v4i16((LAS v4s*)(size_t)a)); }
DEV v4u frag_tr(unsigned a0, unsigned a1) { v2u p = lds_tr(a0), q = lds_tr(a1); v4u r = {p.x, p.y, q.x, q.y}; return r; }
#define PIN(x) asm volatile("" : "+v"(x))

template <int NT> DEV void dma(unsigned ldsdst, unsigned voff, v4i srd, unsigned soff) {
    if (NT) asm volatile("s_mov_b32 m0, %0\n\ts_nop 0\n\tbuffer_load_dwordx4 %1, %2, %3 offen nt lds" ::"s"(ldsdst), "v"(voff), "s"(srd), "s"(soff) : "memory");
    else asm volatile("s_mov_b32 m0, %0\n\ts_nop 0\n\tbuffer_load_dwordx4 %1, %2, %3 offen lds" ::"s"(ldsdst), "v"(voff), "s"(srd), "s"(soff) : "memory");
}
#define WAITV(n) asm volatile("s_waitcnt vmcnt(%0)" ::"n"(n) : "memory")
#define BAR() asm volatile("s_waitcnt lgkmcnt(0)\n\ts_barrier" ::: "memory")
constexpr unsigned OOB = 0x7ff00000u;
DEV void put16(v4i srd, unsigned voff, unsigned a, unsigned b, unsigned tagv) {
    v4u v = {a, tagv, b, tagv};
    asm volatile("buffer_store_dwordx4 %0, %1, %2, 0 offen sc1\n\ts_nop 1" ::"v"(v), "v"(voff), "s"(srd) : "memory");
}
DEV void put8(v4i srd, unsigned voff, unsigned a, unsigned tagv) {
    v2u v = {a, tagv};
    asm volatile("buffer_store_dwordx2 %0, %1, %2, 0 offen sc1\n\ts_nop 1" ::"v"(v), "v"(voff), "s"(srd) : "memory");
}

#define SPINS "0x3000"
#define ST(i) do { } while (0)
#define STARG
#define STPASS

struct Ctx {
    int l, w, gq, c;
    unsigned q4, p4, p8, trsw, tr128;
    unsigned tag, tagv;
    v4i s_ws, s_x;
};

template <int I0, int I1> DEV void mlp_load(const Ctx& k, int fb, const u16* w1, const u16* w2) {
    const v4i s_w1 = mksrd(w1, 1835008u), s_w2 = mksrd(w2, 1835008u);
    const unsigned vo = (unsigned)(k.l >> 2) * 3584u + (unsigned)(((k.l & 3) ^ ((k.l >> 5) << 1)) * 16);
    const unsigned so = (unsigned)(k.w * 64) * 3584u + (unsigned)fb * 64u;
    const unsigned vo2 = (unsigned)(k.l >> 3) * 1024u + k.p8;
    const unsigned so2 = (unsigned)(fb * 32) * 1024u + (unsigned)k.w * 128u;
#pragma unroll
    for (int i = I0; i < I1; ++i) {
        if (i < 4) dma<NTW>(L_R + k.w * 8192 + i * 1024, vo, s_w1, so + i * 16 * 3584);
        else dma<NTW>(L_R + k.w * 8192 + i * 1024, vo2 ^ (unsigned)((i & 1) * 64), s_w2, so2 + (i - 4) * 8192);
    }
}
template <int I0, int I1> DEV void prj_load(const Ctx& k, int gP, int dh, int cb, const u16* wo) {
    const v4i s_wo = mksrd(wo, 4u << 20);
    const unsigned vo = (unsigned)(k.l >> 3) * 1024u + k.p8;
    const unsigned so = (unsigned)(gP * 512 + (k.w >> 2) * 256 + (dh * 4 + (k.w & 3)) * 16) * 1024u + (unsigned)cb * 128u;
    const unsigned lo = L_R + (unsigned)((k.w & 3) * 8192 + (k.w >> 2) * 4096);
#pragma unroll
    for (int i = I0; i < I1; ++i) dma<NTW>(lo + i * 1024, vo ^ (unsigned)((i & 1) * 64), s_wo, so + ((i >> 1) * 128 + (i & 1) * 8) * 1024);
}
DEV void mlp_run(STARG const Ctx& k, int bM, int fb, const u16* w2) {
    const int l = k.l, w = k.w, gq = k.gq, c = k.c;
    const v4f zz = {0.f, 0.f, 0.f, 0.f};
    v4u ones = {0x3f803f80u, 0x3f803f80u, 0x3f803f80u, 0x3f803f80u};
    mlp_load<4, 8>(k, fb, w2, w2);
    WAITV(4);
    ST(13);
    v4u f1[2][2], f2[4];
    float cs;
    {
        const unsigned a = L_R + (unsigned)w * 8192u + ((k.q4 & 1) * 16 + (unsigned)gq * 4 + (k.q4 >> 1)) * 64u + k.p4 * 8u;
        const unsigned sx = (unsigned)(gq >> 1) * 32u;
#pragma unroll
        for (int ks = 0; ks < 2; ++ks) {
            f1[ks][0] = frag_tr(a + sx + ks * 2048, a + sx + ks * 2048 + 128);
            f1[ks][1] = frag_tr(a + (sx ^ 32u) + ks * 2048, a + (sx ^ 32u) + ks * 2048 + 128);
        }
        const v4f d0 = mfma(ones, f1[1][0], mfma(ones, f1[0][0], zz)), d1 = mfma(ones, f1[1][1], mfma(ones, f1[0][1], zz));
        lds_st<float>(L_ZE + (unsigned)w * 128u + (unsigned)(l & 31) * 4u, (gq & 1) ? d1.x : d0.x);
        BAR();
        const unsigned r = L_ZE + (unsigned)(c >> 3) * 512u + (unsigned)(gq * 8 + (c & 7)) * 4u;
        cs = (lds_ld<float>(r) + lds_ld<float>(r + 128)) + (lds_ld<float>(r + 256) + lds_ld<float>(r + 384));
        cs += dppf<XOR8>(cs);
    }
    const unsigned hs = L_C + (unsigned)w * 2048u;
    unsigned ho = WS_H + (unsigned)(bM * 8 + w) * 2048u + (unsigned)(l >> 3) * 128u + (unsigned)(((l & 7) ^ gq) * 16), ho1 = ho ^ 64u;
    unsigned ha = hs + (unsigned)c * 128u + (unsigned)((gq ^ (c >> 1)) * 16), ha1 = ha ^ 64u;
    unsigned za = L_ZR + (unsigned)((gq >> 1) * 16 + c) * 80u + (unsigned)(w * 2 + (gq & 1)) * 4u;
    unsigned ga = L_GS + (unsigned)(w * 16 + gq * 4);
    unsigned zr = L_ZR + (unsigned)(gq * 8 + (c & 7)) * 80u + (unsigned)(c >> 3) * 32u;
    unsigned gr = L_GS + (unsigned)(l & 7) * 16u;
    unsigned yo = (gq & 1) ? OOB : WS_Y + ((unsigned)(bM * 16 + w * 2 + (gq >> 1)) * 56u + (unsigned)fb) * 128u + (unsigned)c * 8u;
    PIN(ho); PIN(ho1); PIN(ha); PIN(ha1); PIN(za); PIN(ga); PIN(zr); PIN(gr); PIN(yo); PIN(cs); PIN(ones);
    WAITV(0);
    ST(14);
    {
        const unsigned a2 = L_R + (unsigned)w * 8192u + 4096u + k.tr128;
#pragma unroll
        for (int nt = 0; nt < 4; ++nt) { const unsigned q = a2 + (((unsigned)nt ^ k.trsw) * 32u); f2[nt] = frag_tr(q, q + 512); }
    }
    ST(7);
    v4u x0, x1;
    {
        unsigned t, t2, cnt;
        asm volatile("s_movk_i32 %[cnt], " SPINS "\n\t"
                     "s_mov_b32 m0, %[hs]\n"
                     "1:\n\t"
                     "buffer_load_dwordx4 %[o0], %[srd], 0 offen sc1 lds\n\t"
                     "buffer_load_dwordx4 %[o1], %[srd], 0 offen offset:1024 sc1 lds\n\t"
                     "s_sub_u32 %[cnt], %[cnt], 1\n\t"
                     "s_waitcnt vmcnt(0)\n\t"
                     "ds_read_b128 v[160:163], %[a0]\n\t"
                     "ds_read_b128 v[164:167], %[a1]\n\t"
                     "s_waitcnt lgkmcnt(0)\n\t"
                     "s_cbranch_scc1 2f\n\t"
                     "v_max3_u32 %[t], v160, v161, v162\n\t"
                     "v_max3_u32 %[t2], v163, v164, v165\n\t"
                     "v_max3_u32 %[t], %[t], v166, v167\n\t"
                     "v_max_u32 %[t], %[t], %[t2]\n\t"
                     "v_cmp_eq_u32 vcc, -1, %[t]\n\t"
                     "s_cbranch_vccnz 1b\n"
                     "2:"
                     : [t] "=&v"(t), [t2] "=&v"(t2), [cnt] "=&s"(cnt), "=&{v[160:163]}"(x0), "=&{v[164:167]}"(x1)
                     : [o0] "v"(ho), [o1] "v"(ho1), [srd] "s"(k.s_ws), [hs] "s"(hs), [a0] "v"(ha), [a1] "v"(ha1) : "memory", "vcc", "scc");
    }
    ST(8);
    {
        v4f gm = mfma(x0, x0, zz), rs = mfma(ones, x0, zz);
        gm = mfma(x1, x1, gm); rs = mfma(ones, x1, rs);
        v4f d0 = mfma(x0, f1[0][0], zz), d1 = mfma(x0, f1[0][1], zz);
        d0 = mfma(x1, f1[1][0], d0); d1 = mfma(x1, f1[1][1], d1);
        lds_st<float>(ga, sum16(mix32((gm.x + gm.y) + (gm.z + gm.w), rs.x)));
        lds_st<float>(za, mix32((d0.x + d0.y) + (d0.z + d0.w), (d1.x + d1.y) + (d1.z + d1.w)));
    }
    BAR();
    {
        const v4f g4 = lds_ld<v4f>(gr);
        const v4f za4 = lds_ld<v4f>(zr), zb4 = lds_ld<v4f>(zr + 16);
        const float s2 = sum8(g4.x + g4.y) * (1.0f / 512.0f), mean = sum8(g4.z) * (1.0f / 1024.0f);
        const v4f zs = za4 + zb4;
        float z = (zs.x + zs.y) + (zs.z + zs.w);
        z += dppf<XOR8>(z);
        const float var = vmax(s2 - mean * mean, 0.f);
        const float mr = rb(mean), den = rb(__builtin_amdgcn_sqrtf(rb(rb(var) + 1e-5f)));
        z = rb((z - mr * cs) * __builtin_amdgcn_rcpf(den));
        const float hv = z * __builtin_amdgcn_rcpf(1.0f + __builtin_amdgcn_exp2f(z * -1.4426950408889634f));
        const unsigned pk = cvtpk(hv, dppf<0x101>(hv));
        const v4u af = {pk, dppu<0x102>(pk), dppu<0x104>(pk), dppu<0x106>(pk)};
        const v4f y0 = mfma(af, f2[0], zz), y2 = mfma(af, f2[2], zz), y1 = mfma(af, f2[1], zz), y3 = mfma(af, f2[3], zz);
        const bool hi = gq >= 2;
        put8(k.s_ws, yo, __float_as_uint(hi ? y2.x : y0.x), __float_as_uint(hi ? y3.x : y1.x));
    }
    ST(9);
}
DEV void fin_run(STARG const Ctx& k, int bF, int cF, int s, u16* out) {
    const int l = k.l, n7 = l & 7;
    const int col = cF * 64 + (s >> 1) * 32 + (s & 1) * 8 + n7;
    unsigned h0, h1;
    {
        const unsigned ho = WS_H + (unsigned)((bF * 8 + cF) * 16 + (l >> 3)) * 128u + (unsigned)(s * 8 + n7) * 4u;
        unsigned t, cnt;
        asm volatile("s_movk_i32 %[cnt], " SPINS "\n"
                     "1:\n\t"
                     "buffer_load_dword %[h0], %[o], %[srd], 0 offen sc1\n\t"
                     "buffer_load_dword %[h1], %[o], %[srd], 0 offen offset:1024 sc1\n\t"
                     "s_sub_u32 %[cnt], %[cnt], 1\n\t"
                     "s_waitcnt vmcnt(0)\n\t"
                     "s_cbranch_scc1 2f\n\t"
                     "v_max_u32 %[t], %[h0], %[h1]\n\t"
                     "v_cmp_eq_u32 vcc, -1, %[t]\n\t"
                     "s_cbranch_vccnz 1b\n"
                     "2:"
                     : [t] "=&v"(t), [cnt] "=&s"(cnt), [h0] "=&v"(h0), [h1] "=&v"(h1) : [o] "v"(ho), [srd] "s"(k.s_ws) : "memory", "vcc", "scc");
    }
    float a = mix32(bf_lo(h0) + bf_lo(h1), bf_hi(h0) + bf_hi(h1));
    a += dppf<XOR8>(a);
    float hh = rb(x16_sum(a));
    unsigned yo = WS_Y + (unsigned)(bF * 16 + cF * 2 + (s >> 1)) * 7168u + (unsigned)(l >> 3) * 128u + (unsigned)((s & 1) * 64 + n7 * 8), yo2 = yo + 4096u;
    const v4i s_out = mksrd(out, 2048);
    unsigned oo = (l & 24) ? OOB : (unsigned)(bF * 512 + col) * 2u + (unsigned)(l >> 5) * 32u;
    PIN(hh); PIN(yo); PIN(yo2); PIN(oo);
    ST(7);
    v2f y0, y1, y2, y3, y4, y5, y6;
    unsigned t, cnt;
    asm volatile("s_movk_i32 %[cnt], " SPINS "\n"
                 "1:\n\t"
                 "buffer_load_dwordx2 v[160:161], %[o1], %[srd], 0 offen sc1\n\t"
                 "buffer_load_dwordx2 v[162:163], %[o1], %[srd], 0 offen offset:1024 sc1\n\t"
                 "buffer_load_dwordx2 v[164:165], %[o1], %[srd], 0 offen offset:2048 sc1\n\t"
                 "buffer_load_dwordx2 v[166:167], %[o1], %[srd], 0 offen offset:3072 sc1\n\t"
                 "buffer_load_dwordx2 v[168:169], %[o2], %[srd], 0 offen sc1\n\t"
                 "buffer_load_dwordx2 v[170:171], %[o2], %[srd], 0 offen offset:1024 sc1\n\t"
                 "buffer_load_dwordx2 v[172:173], %[o2], %[srd], 0 offen offset:2048 sc1\n\t"
                 "s_sub_u32 %[cnt], %[cnt], 1\n\t"
                 "s_waitcnt vmcnt(4)\n\t"
                 "v_max3_u32 %[t], v161, v163, v165\n\t"
                 "s_waitcnt vmcnt(2)\n\t"
                 "v_max3_u32 %[t], %[t], v167, v169\n\t"
                 "s_waitcnt vmcnt(0)\n\t"
                 "s_cbranch_scc1 2f\n\t"
                 "v_max3_u32 %[t], %[t], v171, v173\n\t"
                 "v_cmp_eq_u32 vcc, -1, %[t]\n\t"
                 "s_cbranch_vccnz 1b\n"
                 "2:"
                 : [t] "=&v"(t), [cnt] "=&s"(cnt), "=&{v[160:161]}"(y0), "=&{v[162:163]}"(y1), "=&{v[164:165]}"(y2),
                   "=&{v[166:167]}"(y3), "=&{v[168:169]}"(y4), "=&{v[170:171]}"(y5), "=&{v[172:173]}"(y6)
                 : [o1] "v"(yo), [o2] "v"(yo2), [srd] "s"(k.s_ws) : "memory", "vcc", "scc");
    ST(8);
    const v2f ys = ((y0 + y1) + (y2 + y3)) + ((y4 + y5) + y6);
    float y = mix32(ys.x, ys.y);
    y += dppf<XOR8>(y);
    y = x16_sum(y);
    const unsigned r = cvtpk(hh + rb(y), 0.f);
    asm volatile("buffer_store_short %0, %1, %2, 0 offen" ::"v"(r), "v"(oo), "s"(s_out) : "memory");
    if ((bF | cF | s) == 0) {
        const unsigned zo = l ? OOB : 0u;
        asm volatile("buffer_store_dword %0, %1, %2, 0 offen sc1" ::"v"(k.tagv), "v"(zo), "s"(k.s_ws) : "memory");
    }
    ST(9);
}

__global__ void __launch_bounds__(512, 2)
fused_block(const u16* __restrict__ x, const u16* __restrict__ kc, const u16* __restrict__ vc, const u16* __restrict__ wq,
            const u16* __restrict__ wo, const u16* __restrict__ w1, const u16* __restrict__ w2, u16* __restrict__ out,
            unsigned char* __restrict__ ws) {
    __shared__ __attribute__((aligned(1024))) unsigned char lds_raw[L_END];
    asm volatile("" ::"v"((LAS unsigned char*)lds_raw));
    const int tid = threadIdx.x, l = tid & 63;
    const int w = __builtin_amdgcn_readfirstlane(tid >> 6);
    const int xc = blockIdx.x & 7, u = blockIdx.x >> 3;
    const int gq = l >> 4, c = l & 15;
#define QM 0x65432107
    const int kq = u & 3, cq = u >> 2, gQ = (QM >> (xc * 4)) & 7;
    const int bA = u >> 4, gA = (xc + 7) & 7, s = u & 15;
#define XR 0x06543217
    const int xr = (XR >> (xc * 4)) & 7;
    const bool prj = xr < 4, mlp = s < 14;
    const int gP = xr * 2 + ((u >> 3) & 1), cb = u & 7, dh = u >> 4;
    const int fb = (xr - 4) * 14 + (s < 14 ? s : 13);
    Ctx k;
    k.l = l; k.w = w; k.gq = gq; k.c = c;
    k.s_ws = mksrd(ws, WS_END); k.s_x = mksrd(x, 2048);
    unsigned ev;
    asm volatile("s_load_dword %0, %1, 0x0" : "=s"(ev) : "s"(ws), "s"(kc), "s"(vc), "s"(wq), "s"(wo), "s"(w1), "s"(w2), "s"(out) : "memory");

    unsigned xv; v4u xa;
    {
        const unsigned xo = (unsigned)((w >> 1) & 1) * 1024u + (unsigned)l * 16u, xvo = (unsigned)(((tid >> 7) & 1) * 512 + kq * 128 + (tid & 127)) * 2u;
        asm volatile("buffer_load_dwordx4 %0, %2, %3, 0 offen\n\t"
                     "buffer_load_ushort %1, %4, %3, 0 offen"
                     : "=&{v[160:163]}"(xa), "=&{v164}"(xv) : "v"(xo), "s"(k.s_x), "v"(xvo) : "memory");
    }
    k.p8 = (unsigned)(((l & 7) ^ (((l >> 4) & 1) << 1)) * 16);
    {
        const v4i s_wq = mksrd(wq, 4u << 20);
        const unsigned vo = (unsigned)(l >> 3) * 8192u + k.p8;
        const unsigned so = (unsigned)(kq * 128 + w * 16) * 8192u + (unsigned)(gQ * 512 + cq * 64) * 2u;
        dma<NTA>(L_C + w * 2048, vo, s_wq, so);
        dma<NTA>(L_C + w * 2048 + 1024, vo ^ 64u, s_wq, so + 8u * 8192u);
    }
    if (w == 5) {
        const unsigned c0 = WS_O + (unsigned)(bA * 8 + gA) * 16384u + (unsigned)s * 128u + (unsigned)(l >> 3) * 2048u + (unsigned)(l & 7) * 16u;
        const unsigned c1 = l < 56 ? WS_Y + blockIdx.x * 896u + (unsigned)l * 16u : OOB;
        const unsigned c2 = ((prj && u < 8) ? WS_H + (unsigned)(xr * 8 + u) * 1024u : OOB) + (unsigned)l * 16u;
        const v4u m1 = {~0u, ~0u, ~0u, ~0u};
        asm volatile("buffer_store_dwordx4 %0, %1, %4, 0 offen sc1\n\t"
                     "buffer_store_dwordx4 %0, %2, %4, 0 offen sc1\n\t"
                     "buffer_store_dwordx4 %0, %3, %4, 0 offen sc1\n\ts_nop 1" ::"v"(m1), "v"(c0), "v"(c1), "v"(c2), "s"(k.s_ws) : "memory");
    }
#define WAIT5(n) "s_cmp_eq_u32 %[w], 5\n\ts_cbranch_scc1 1f\n\ts_waitcnt vmcnt(" #n ")\n1:\n\ts_waitcnt vmcnt(" #n " + 3)"
    const v4i s_k = mksrd(kc, 32u << 20), s_v = mksrd(vc, 32u << 20);
    const unsigned kvb = (unsigned)((bA * 8 + gA) * 8192 + 6144 + s * 128 + w * 16) * 256u;
    unsigned vvo = (unsigned)gq * 256u + (unsigned)((c ^ (gq << 1)) * 16);
    unsigned kvo = (unsigned)gq * 256u + (unsigned)((c ^ gq) * 16);
#define KLD(i) dma<NTA>(L_K + (w * 4 + (i)) * 1024, kvo ^ (unsigned)((i) * 64), s_k, kvb + (i) * 1024)
#define VLD(i) dma<NTA>(L_V + (w * 4 + (i)) * 1024, vvo ^ (unsigned)(((i) & 1) * 128), s_v, kvb + (i) * 1024)
    k.q4 = (unsigned)((l >> 2) & 3); k.p4 = (unsigned)(l & 3);
    k.trsw = (k.q4 >> 1) | ((unsigned)(gq & 1) << 1);
    k.tr128 = (unsigned)(gq * 8 + k.q4) * 128u + k.p4 * 8u;
    unsigned qa = L_C + k.tr128 + (((unsigned)w ^ k.trsw) * 32u);
    unsigned la = L_LN + (unsigned)((c >> 2) & 1) * 320u + (unsigned)gq * 16u;
    unsigned qoff = (l < 32 && w < 4) ? WS_Q + (unsigned)((((gQ * 2 + gq) * 4 + kq) * 512) + cq * 64 + w * 16 + c) * 8u
                  : tid == 320 ? WS_F + (unsigned)(xr * 32 + u) * 4u : OOB;
    PIN(qa); PIN(la); PIN(qoff);
    asm volatile(WAIT5(2) : "+{v[160:163]}"(xa), "+{v164}"(xv) : [w] "s"(w) : "memory", "scc");
    if (w < 4) {
        const float s1 = dot2(xa.w, 0x3f803f80u, dot2(xa.z, 0x3f803f80u, dot2(xa.y, 0x3f803f80u, dot2(xa.x, 0x3f803f80u, 0.f))));
        const float s2 = dot2(xa.w, xa.w, dot2(xa.z, xa.z, dot2(xa.y, xa.y, dot2(xa.x, xa.x, 0.f))));
        const float t = x16_sum(sum16(mix32(s1, s2))) * (1.0f / 512.0f);
        const float mean = __builtin_bit_cast(float, __builtin_amdgcn_readlane(__builtin_bit_cast(int, t), 0));
        const float msq = __builtin_bit_cast(float, __builtin_amdgcn_readlane(__builtin_bit_cast(int, t), 32));
        const float var = vmax(msq - mean * mean, 0.f);
        const float mr = rb(mean), den = rb(__builtin_amdgcn_sqrtf(rb(rb(var) + 1e-5f)));
        const float ln = __fdiv_rn(rb(bf_lo(xv) - mr), den);
        lds_st<u16>(L_LN + (tid >> 7) * 320 + (tid & 127) * 2, (u16)cvtpk(ln, ln));
    } else if (w == 4) {
        lds_st<unsigned>(L_Z + l * 4, l < 48 ? 0u : 0xff800000u);
    } else if (w < 7) {
        lds_st<v4u>(L_X + (unsigned)(w - 5) * 1024u + (unsigned)l * 16u, xa);
    }
    asm volatile(WAIT5(1) :: [w] "s"(w) : "memory", "scc");
    KLD(0);
    asm volatile(WAIT5(1) :: [w] "s"(w) : "memory", "scc");
    asm volatile("s_waitcnt lgkmcnt(0)\n\ts_barrier" : "+s"(ev) :: "memory");
    KLD(1);
    ST(1);
    k.tag = ev + 1u;
    k.tagv = k.tag;
    PIN(k.tagv);
    {
        if (w < 4) {
            const v4f zz = {0.f, 0.f, 0.f, 0.f};
            v4u bw[4], al[4];
#pragma unroll
            for (int ks = 0; ks < 4; ++ks) { bw[ks] = frag_tr(qa + ks * 4096, qa + ks * 4096 + 512); al[ks] = lds_ld<v4u>(la + ks * 64); }
            __builtin_amdgcn_sched_barrier(0);
            v4f acc = mfma(al[0], bw[0], zz);
#pragma unroll
            for (int ks = 1; ks < 4; ++ks) acc = mfma(al[ks], bw[ks], acc);
            put8(k.s_ws, qoff, __float_as_uint(acc.x), k.tagv);
        } else {
            asm volatile("s_waitcnt vmcnt(2)\n\tbuffer_store_dword %0, %1, %2, 0 offen sc1" ::"v"(k.tagv), "v"(qoff), "s"(k.s_ws) : "memory");
        }
    }
    ST(2);

    {
        unsigned qo = WS_Q + (unsigned)(gA * 2 + bA) * 16384u + (unsigned)(l >> 5) * 8192u + (unsigned)w * 512u + (unsigned)(l & 31) * 16u;
        PIN(qo);
        WAITV(2);
        KLD(2);
        WAITV(2);
        KLD(3);
        unsigned qw = L_QI + (unsigned)(w >> 1) * 320u + (unsigned)(w & 1) * 128u + (unsigned)(l & 31) * 4u + (unsigned)(l >> 5) * 2u;
        unsigned aq = L_QI + (unsigned)(c >> 2) * 320u + (unsigned)gq * 16u;
        unsigned mxa = L_MX + (unsigned)gq * 16u;
        unsigned mxw = mxa + (unsigned)(c & 3) * 4u;
        unsigned pr = L_P + (unsigned)gq * 64u + (unsigned)(c & 3) * 16u;
        unsigned pw = L_P + (unsigned)(w >> 1) * 256u + (unsigned)(c >> 2) * 64u + (unsigned)gq * 16u + (unsigned)((w & 1) * 4 + (c & 3)) * 2u;
        const unsigned vrow = (unsigned)gq * 4u + k.q4;
        unsigned va = L_V + vrow * 256u + k.p4 * 8u + (((unsigned)w ^ (vrow & 7u)) * 32u);
        unsigned oo = l < 16 ? WS_O + (unsigned)(bA * 8 + gA) * 16384u + (unsigned)w * 2048u + (unsigned)s * 128u + (unsigned)l * 8u : OOB;
        unsigned ck = WS_F + ((prj && l < 32) ? (unsigned)((XR >> (((gP + 1) & 7) * 4)) & 7) * 128u + (unsigned)l * 4u
                                                   : (unsigned)(l & 3) * 128u + (unsigned)((l >> 2) & 7) * 4u);
        PIN(qw); PIN(aq); PIN(mxa); PIN(mxw); PIN(pr); PIN(pw); PIN(va); PIN(oo); PIN(ck);
        unsigned vvo1 = vvo ^ 128u;
        PIN(vvo); PIN(vvo1);
        WAITV(1);
        ST(3);
        v4u g0, g1;
        const unsigned k4 = 4096u;
        asm volatile("buffer_load_dwordx4 %0, %2, %3, 0 offen sc1\n\t"
                     "buffer_load_dwordx4 %1, %2, %3, %4 offen sc1"
                     : "=&{v[160:163]}"(g0), "=&{v[164:167]}"(g1) : "v"(qo), "s"(k.s_ws), "s"(k4) : "memory");
        VLD(0);
        WAITV(3);
        VLD(1);
        v4u kf[4];
#pragma unroll
        for (int st = 0; st < 4; ++st) kf[st] = lds_ld<v4u>(L_K + (unsigned)(w * 16 + c) * 256u + (unsigned)(((st * 4 + gq) ^ c) * 16));
        {
            unsigned t, t2, cnt;
#define NTS " nt"
            asm volatile("s_movk_i32 %[cnt], " SPINS "\n\t"
                         "s_mov_b32 m0, %[m2]\n\t"
                         "s_waitcnt vmcnt(2)\n\t"
                         "v_bitop3_b32 %[t], v161, v163, %[tag] bitop3:0x7e\n\t"
                         "v_bitop3_b32 %[t2], v165, v167, %[tag] bitop3:0x7e\n\t"
                         "v_or_b32 %[t], %[t], %[t2]\n\t"
                         "v_cmp_ne_u32 vcc, 0, %[t]\n\t"
                         "s_cbranch_vccz 2f\n\t"
                         "buffer_load_dwordx4 v[160:163], %[o1], %[srd], 0 offen sc1\n\t"
                         "buffer_load_dwordx4 v[164:167], %[o1], %[srd], %[k4] offen sc1\n\t"
                         "s_sub_u32 %[cnt], %[cnt], 1\n"
                         "2:\n\t"
                         "buffer_load_dwordx4 %[v2], %[sv], %[kvb] offen offset:2048" NTS " lds\n\t"
                         "buffer_load_dwordx4 %[v3], %[sv], %[kvb] offen offset:3072" NTS " lds\n\t"
                         "s_cbranch_vccz 3f\n\t"
                         "s_waitcnt vmcnt(2)\n"
                         "1:\n\t"
                         "v_bitop3_b32 %[t], v161, v163, %[tag] bitop3:0x7e\n\t"
                         "v_bitop3_b32 %[t2], v165, v167, %[tag] bitop3:0x7e\n\t"
                         "v_or_b32 %[t], %[t], %[t2]\n\t"
                         "v_cmp_ne_u32 vcc, 0, %[t]\n\t"
                         "s_cbranch_vccz 3f\n\t"
                         "buffer_load_dwordx4 v[160:163], %[o1], %[srd], 0 offen sc1\n\t"
                         "buffer_load_dwordx4 v[164:167], %[o1], %[srd], %[k4] offen sc1\n\t"
                         "s_sub_u32 %[cnt], %[cnt], 1\n\t"
                         "s_waitcnt vmcnt(0)\n\t"
                         "s_cbranch_scc0 1b\n"
                         "3:"
                         : [t] "=&v"(t), [t2] "=&v"(t2), [cnt] "=&s"(cnt), "+{v[160:163]}"(g0), "+{v[164:167]}"(g1)
                         : [o1] "v"(qo), [srd] "s"(k.s_ws), [k4] "s"(k4), [tag] "s"(k.tag), [m2] "s"(L_V + w * 4096), [v2] "v"(vvo), [v3] "v"(vvo1),
                           [sv] "s"(s_v), [kvb] "s"(kvb)
                         : "memory", "vcc", "scc");
            const float qv = mix32(__uint_as_float(g0.x) + __uint_as_float(g1.x), __uint_as_float(g0.z) + __uint_as_float(g1.z));
            lds_st<u16>(qw, (u16)cvtpk(qv, qv));
        }
        BAR();
        ST(4);
        float s0;
        {
            v4u qf[4];
#pragma unroll
            for (int st = 0; st < 4; ++st) qf[st] = lds_ld<v4u>(aq + st * 64);
            __builtin_amdgcn_sched_barrier(0);
            v4f sc = {0.f, 0.f, 0.f, 0.f};
#pragma unroll
            for (int st = 0; st < 4; ++st) sc = mfma(qf[st], kf[st], sc);
            s0 = rb(rb(sc.x) * 0.08838834764831845f);
            __builtin_amdgcn_ds_fmaxf((LAS float*)(size_t)mxw, s0, 0, 0, false);
        }
        BAR();
        {
            const v4f m4 = lds_ld<v4f>(mxa);
            const float mx = vmax3(m4.x, m4.y, vmax(m4.z, m4.w));
            const float e0 = __builtin_amdgcn_exp2f((s0 - mx) * 1.4426950408889634f);
            lds_st<u16>(pw, (u16)cvtpk(e0, e0));
        }
        BAR();
        v4u pf[4];
#pragma unroll
        for (int j = 0; j < 4; ++j) pf[j] = lds_ld<v4u>(pr + j * 256);
        unsigned ct;
        asm volatile("s_cmp_lg_u32 %1, 6\n\ts_cbranch_scc1 1f\n\tbuffer_load_dword %0, %2, %3, 0 offen sc1\n1:"
                     : "=&{v168}"(ct) : "s"(w), "v"(ck), "s"(k.s_ws) : "memory", "scc");
        if (w == 7) {
            const v4f m4 = lds_ld<v4f>(L_MX + (unsigned)(c & 3) * 16u);
            const float mx = vmax3(m4.x, m4.y, vmax(m4.z, m4.w));
            const v4u ones = {0x3f803f80u, 0x3f803f80u, 0x3f803f80u, 0x3f803f80u};
            v4f ls = {0.f, 0.f, 0.f, 0.f};
#pragma unroll
            for (int j = 0; j < 4; ++j) ls = mfma(ones, pf[j], ls);
            put16(k.s_ws, l < 8 ? WS_ML + (unsigned)((bA * 8 + gA) * 16 + s) * 128u + (unsigned)l * 16u : OOB, __float_as_uint(mx), __float_as_uint(ls.x), k.tagv);
        }
        ST(12);
        if (prj) {
            prj_load<0, REP>(k, gP, dh, cb, wo);
            asm volatile("s_cmp_gt_u32 %0, 5\n\ts_cbranch_scc1 1f\n\ts_waitcnt vmcnt(%1)\n1:\n\ts_waitcnt vmcnt(%2)" :: "s"(w), "n"(REP), "n"(REP + 1) : "memory", "scc");
        } else {
            mlp_load<0, REM>(k, fb, w1, w2);
            asm volatile("s_cmp_gt_u32 %0, 5\n\ts_cbranch_scc1 1f\n\ts_waitcnt vmcnt(%1)\n1:\n\ts_waitcnt vmcnt(%2)" :: "s"(w), "n"(REM), "n"(REM + 1) : "memory", "scc");
        }
        asm volatile("s_barrier" ::: "memory");
        ST(5);
        {
            v4u vf[4];
#pragma unroll
            for (int j = 0; j < 4; ++j) vf[j] = frag_tr(va + j * 8192, va + j * 8192 + 4096);
            __builtin_amdgcn_sched_barrier(0);
            v4f o = {0.f, 0.f, 0.f, 0.f};
#pragma unroll
            for (int j = 0; j < 4; ++j) o = mfma(pf[j], vf[j], o);
            put8(k.s_ws, oo, cvtpk(o.x, o.y), cvtpk(o.z, o.w));
        }
        {
            unsigned cnt;
            asm volatile("s_cmp_lg_u32 %[w], 6\n\t"
                         "s_cbranch_scc1 3f\n\t"
                         "s_movk_i32 %[cnt], " SPINS "\n\t"
                         "s_cmp_lt_u32 %[pj], 4\n\t"
                         "s_cbranch_scc1 4f\n\t"
                         "s_waitcnt vmcnt(%[nm])\n\t"
                         "s_branch 1f\n"
                         "4:\n\t"
                         "s_waitcnt vmcnt(%[np])\n"
                         "1:\n\t"
                         "v_cmp_ne_u32 vcc, %[tag], v168\n\t"
                         "s_cbranch_vccz 3f\n\t"
                         "buffer_load_dword v168, %[ck], %[srd], 0 offen sc1\n\t"
                         "s_sub_u32 %[cnt], %[cnt], 1\n\t"
                         "s_waitcnt vmcnt(0)\n\t"
                         "s_cbranch_scc0 1b\n"
                         "3:\n\t"
                         "s_barrier"
                         : [cnt] "=&s"(cnt), "+{v168}"(ct)
                         : [w] "s"(w), [pj] "s"(xr), [nm] "n"(REM + 1), [np] "n"(REP + 1), [tag] "s"(k.tag), [ck] "v"(ck), [srd] "s"(k.s_ws)
                         : "memory", "vcc", "scc");
        }
        ST(6);
    }
    if (!prj) {
        if (mlp) {
            if (MD) __builtin_amdgcn_s_sleep(MD);
            mlp_load<REM, 4>(k, fb, w1, w2);
            mlp_run(STPASS k, bA, fb, w2);
        }
    } else {
        prj_load<REP, 4>(k, gP, dh, cb, wo);
        const int bP = w >> 2;
        unsigned oo = WS_O + (unsigned)(bP * 8 + gP) * 16384u + (unsigned)(dh * 4 + (w & 3)) * 2048u + (unsigned)l * 8u;
        unsigned mo = WS_ML + (unsigned)((bP * 8 + gP) * 16 + (c >> 2) * 4 + gq) * 128u + (unsigned)(c & 3) * 16u;
        PIN(oo); PIN(mo);
        v4u ml;
        v2u o0, o1, o2, o3;
        if (PW < 63) WAITV(PW);
#define OLDS "buffer_load_dwordx2 v[160:161], %[off], %[srd], 0 offen sc1\n\t"             \
             "buffer_load_dwordx2 v[162:163], %[off], %[srd], 0 offen offset:512 sc1\n\t"  \
             "buffer_load_dwordx2 v[164:165], %[off], %[srd], 0 offen offset:1024 sc1\n\t" \
             "buffer_load_dwordx2 v[166:167], %[off], %[srd], 0 offen offset:1536 sc1\n\t"
        asm volatile("buffer_load_dwordx4 v[176:179], %[mo], %[srd], 0 offen sc1\n\t" OLDS
                     : "=&{v[160:161]}"(o0), "=&{v[162:163]}"(o1), "=&{v[164:165]}"(o2), "=&{v[166:167]}"(o3), "=&{v[176:179]}"(ml)
                     : [off] "v"(oo), [mo] "v"(mo), [srd] "s"(k.s_ws) : "memory");
        unsigned ow = L_OS + (unsigned)w * 128u + (unsigned)l * 2u;
        unsigned ar[4];
#pragma unroll
        for (int t = 0; t < 4; ++t) { ar[t] = (c & 3) == t ? L_OS + (unsigned)w * 128u + (unsigned)gq * 16u : L_Z; PIN(ar[t]); }
        unsigned hw = L_HR + (unsigned)w * 256u + (unsigned)c * 16u;
        unsigned hr = hw + (unsigned)gq * 256u;
        unsigned hoff = (gq & 1) ? OOB : WS_H + (unsigned)((bP * 8 + cb) * 16 + gP * 2 + dh) * 128u + (unsigned)(gq >> 1) * 64u + (unsigned)c * 4u;
        v4f acc;
        {
            const unsigned xr = (((gP | dh | (w & 3)) == 0) ? L_X + (unsigned)bP * 1024u + (unsigned)cb * 128u : L_Z) + (unsigned)c * 2u;
            acc.x = bf_lo(lds_ld<u16>(xr)); acc.y = bf_lo(lds_ld<u16>(xr + 32)); acc.z = bf_lo(lds_ld<u16>(xr + 64)); acc.w = bf_lo(lds_ld<u16>(xr + 96));
        }
        PIN(ow); PIN(hw); PIN(hr); PIN(hoff); PIN(acc);
        asm volatile("s_waitcnt vmcnt(5)\n\ts_barrier" ::: "memory");
        if (w != 2) ST(7);
        v4u wf[4][2];
        {
            const unsigned a = L_R + (unsigned)(w & 3) * 8192u + k.tr128;
#pragma unroll
            for (int t = 0; t < 4; ++t)
#pragma unroll
                for (int ks = 0; ks < 2; ++ks) { const unsigned q = a + (((unsigned)t ^ k.trsw) * 32u) + ks * 4096; wf[t][ks] = frag_tr(q, q + 512); }
        }
        {
            unsigned t, cnt;
            asm volatile("s_movk_i32 %[cnt], " SPINS "\n"
                         "1:\n\t"
                         "s_waitcnt vmcnt(4)\n\t"
                         "v_bitop3_b32 %[t], v177, v179, %[tag] bitop3:0x7e\n\t"
                         "v_cmp_ne_u32 vcc, 0, %[t]\n\t"
                         "s_cbranch_vccz 2f\n\t"
                         "s_sub_u32 %[cnt], %[cnt], 1\n\t"
                         "s_cbranch_scc1 2f\n\t"
                         "buffer_load_dwordx4 v[176:179], %[mo], %[srd], 0 offen sc1\n\t" OLDS
                         "s_branch 1b\n"
                         "2:\n\t"
                         "s_waitcnt vmcnt(0)\n\t"
                         "v_max3_u32 %[t], v161, v163, v165\n\t"
                         "v_max_u32 %[t], %[t], v167\n\t"
                         "v_cmp_eq_u32 vcc, -1, %[t]\n\t"
                         "s_cbranch_vccz 3f\n\t" OLDS
                         "3:"
                         : [t] "=&v"(t), [cnt] "=&s"(cnt), "+{v[160:161]}"(o0), "+{v[162:163]}"(o1), "+{v[164:165]}"(o2), "+{v[166:167]}"(o3), "+{v[176:179]}"(ml)
                         : [off] "v"(oo), [mo] "v"(mo), [srd] "s"(k.s_ws), [tag] "s"(k.tag) : "memory", "vcc", "scc");
        }
        ST(13);
        float wt;
        {
            const float m = __uint_as_float(ml.x);
            float M = vmax(m, dppf<XOR8>(m));
            M = x32_max(x16_max(vmax(M, dppf<0x124>(M))));
            const float f = __builtin_amdgcn_exp2f((m - M) * 1.4426950408889634f);
            float L = f * __uint_as_float(ml.z);
            L += dppf<XOR8>(L);
            L += dppf<0x124>(L);
            L = x32_sum(x16_sum(L));
            wt = f * __builtin_amdgcn_rcpf(L);
        }
#define WB(n) dppf<0x150 + (n)>(wt)
        v2f W0 = {WB(0), WB(1)}, W1 = {WB(2), WB(3)}, W2 = {WB(4), WB(5)}, W3 = {WB(6), WB(7)};
        v2f W4 = {WB(8), WB(9)}, W5 = {WB(10), WB(11)}, W6 = {WB(12), WB(13)}, W7 = {WB(14), WB(15)};
        PIN(W0); PIN(W1); PIN(W2); PIN(W3); PIN(W4); PIN(W5); PIN(W6); PIN(W7);
#pragma unroll
        for (int t = 0; t < 4; ++t) { PIN(wf[t][0]); PIN(wf[t][1]); }
        {
            unsigned t, cnt;
            asm volatile("s_movk_i32 %[cnt], " SPINS "\n"
                         "1:\n\t"
                         "s_waitcnt vmcnt(0)\n\t"
                         "v_max3_u32 %[t], v161, v163, v165\n\t"
                         "v_max_u32 %[t], %[t], v167\n\t"
                         "v_cmp_eq_u32 vcc, -1, %[t]\n\t"
                         "s_cbranch_vccz 2f\n\t"
                         "s_sub_u32 %[cnt], %[cnt], 1\n\t"
                         "s_cbranch_scc1 2f\n\t" OLDS
                         "s_branch 1b\n"
                         "2:"
                         : [t] "=&v"(t), [cnt] "=&s"(cnt), "+{v[160:161]}"(o0), "+{v[162:163]}"(o1), "+{v[164:165]}"(o2), "+{v[166:167]}"(o3)
                         : [off] "v"(oo), [srd] "s"(k.s_ws) : "memory", "vcc", "scc");
            ST(15);
#define UP(v) (v2f){bf_lo(v), bf_hi(v)}
            v2f a01 = UP(o0.x) * W0, a23 = UP(o0.y) * W1;
            a01 = __builtin_elementwise_fma(UP(o1.x), W2, a01); a23 = __builtin_elementwise_fma(UP(o1.y), W3, a23);
            a01 = __builtin_elementwise_fma(UP(o2.x), W4, a01); a23 = __builtin_elementwise_fma(UP(o2.y), W5, a23);
            a01 = __builtin_elementwise_fma(UP(o3.x), W6, a01); a23 = __builtin_elementwise_fma(UP(o3.y), W7, a23);
            const float v0 = a01.x, v1 = a01.y, v2 = a23.x, v3 = a23.y;
            const float oc = mix16(mix32(v0, v2), mix32(v1, v3));
            lds_st<u16>(ow, (u16)cvtpk(oc, oc));
            v4u af[4][2];
#pragma unroll
            for (int t = 0; t < 4; ++t) { af[t][0] = lds_ld<v4u>(ar[t]); af[t][1] = lds_ld<v4u>(ar[t] + 64); }
            __builtin_amdgcn_sched_barrier(0);
#pragma unroll
            for (int t = 0; t < 4; ++t) { acc = mfma(af[t][0], wf[t][0], acc); acc = mfma(af[t][1], wf[t][1], acc); }
            lds_st<v4f>(hw, acc);
        }
        BAR();
        if ((w & 3) == 0) {
            const v4f t = lds_ld<v4f>(hr);
            const float a = x16_sum(mix32(t.x, t.z)), bq = x16_sum(mix32(t.y, t.w));
            const unsigned hv = cvtpk(a, bq);
            asm volatile("buffer_store_dword %0, %1, %2, 0 offen sc1" ::"v"(hv), "v"(hoff), "s"(k.s_ws) : "memory");
            ST(14);
        }
        if (w == 2 && (u & 1) == 0) fin_run(STPASS k, xr >> 1, (xr & 1) * 4 + (u >> 3), (u >> 1) & 3, out);
    }
}

extern "C" void kernel_launch(void* const* d_in, const int* in_sizes, int n_in, void* d_out, int out_size, void* d_ws, size_t ws_size,
                              hipStream_t stream) {
    fused_block<<<256, 512, 0, stream>>>((const u16*)d_in[0], (const u16*)d_in[1], (const u16*)d_in[2], (const u16*)d_in[3], (const u16*)d_in[4],
                                         (const u16*)d_in[6], (const u16*)d_in[7], (u16*)d_out, (unsigned char*)d_ws);
}
```
